```python
import torch

def _softmax(x, axis=-1):
    return torch.softmax(x, dim=axis)

def _gelu(x):
    return torch.nn.functional.gelu(x, approximate='tanh')

def _silu(x):
    return torch.nn.functional.silu(x)

def _sigmoid(x):
    return torch.sigmoid(x)

def _layernorm(x, axis=-1, eps=1e-05):
    m = x.mean(dim=axis, keepdim=True)
    v = x.var(dim=axis, keepdim=True, unbiased=False)
    return (x - m) / torch.sqrt(v + eps)

def _scatter_add(tgt, idx, src, axis=0):
    return tgt.clone().index_add_(axis, idx, src)

def _segment_sum(src, seg, n):
    out = torch.zeros((n,) + src.shape[1:], dtype=src.dtype)
    return out.index_add_(0, seg, src)

def _pad(x, pw):
    flat = tuple((v for pair in reversed(pw) for v in pair))
    return torch.nn.functional.pad(x, flat)
kernel_name = 'adaLN_cond__compressed_kv_cla__glu_gated_glu__decode'

def setup_inputs():

    _w = lambda a: (lambda t: t.to(torch.bfloat16) if t.is_floating_point() else t)(torch.as_tensor(a))

    def _A_setup():
        B, H, Sq, Sk, D, M = (2, 32, 1, 4096, 128, 640)
        Dc = 4 * D
        return {'x': _w((lambda *s: torch.randn(*s, dtype=torch.bfloat16) * 0.05)(B, Sq, M)), 'c_kv': _w((lambda *s: torch.randn(*s, dtype=torch.bfloat16) * 1.6)(B, Sk, Dc)), 'w_q': _w((lambda *s: torch.randn(*s, dtype=torch.bfloat16) * 0.8)(M, H, D) * M ** (-0.5)), 'w_uk': _w((lambda *s: torch.randn(*s, dtype=torch.bfloat16) * 0.8)(Dc, H, D) * Dc ** (-0.5)), 'w_uv': _w((lambda *s: torch.randn(*s, dtype=torch.bfloat16) * 1.6)(Dc, H, D) * Dc ** (-0.5)), 'w_o': _w((lambda *s: torch.randn(*s, dtype=torch.bfloat16) * 1.6)(H, D, M) * (H * D) ** (-0.5))}
    _A_in = _A_setup()

    def _B_setup():
        B, S, D, F = (2, 1, 640, 1728)
        return {'x': _w((lambda *s: torch.randn(*s, dtype=torch.bfloat16) * 0.05)(B, S, D)), 'w_gate': _w((lambda *s: torch.randn(*s, dtype=torch.bfloat16) * 0.05)(D, F)), 'w_up': _w((lambda *s: torch.randn(*s, dtype=torch.bfloat16) * 0.05)(D, F)), 'w_down': _w((lambda *s: torch.randn(*s, dtype=torch.bfloat16) * 0.05)(F, D))}
    _B_in = _B_setup()
    return {'A_x': _A_in['x'], 'A_c_kv': _A_in['c_kv'], 'A_w_q': _A_in['w_q'], 'A_w_uk': _A_in['w_uk'], 'A_w_uv': _A_in['w_uv'], 'A_w_o': _A_in['w_o'], 'B_x': _B_in['x'], 'B_w_gate': _B_in['w_gate'], 'B_w_up': _B_in['w_up'], 'B_w_down': _B_in['w_down'], 'c': _w((lambda *s: torch.randn(*s, dtype=torch.bfloat16) * 0.1)(2, 640) * 8.0), 'w_mod': _w((lambda *s: torch.randn(*s, dtype=torch.bfloat16) * 0.1)(640, 6 * 640) * 640 ** (-0.5) * 8.0)}

def reference(A_x, A_c_kv, A_w_q, A_w_uk, A_w_uv, A_w_o, B_x, B_w_gate, B_w_up, B_w_down, c, w_mod):

    def _A_ref(x, c_kv, w_q, w_uk, w_uv, w_o):
        B, Sq, M = (x.shape[0], x.shape[1], x.shape[2])
        Sk = c_kv.shape[1]
        H, D = (w_q.shape[1], w_q.shape[2])
        q = torch.einsum('bqm,mhd->bhqd', x, w_q)
        k = torch.einsum('bkc,chd->bhkd', c_kv, w_uk)
        v = torch.einsum('bkc,chd->bhkd', c_kv, w_uv)
        scores = torch.einsum('bhqd,bhkd->bhqk', q, k) * D ** (-0.5)
        off = Sk - Sq
        qi = torch.arange(Sq)[:, None]
        kj = torch.arange(Sk)[None, :]
        cmask = kj <= qi + off
        scores = torch.where(cmask[None, None], scores, -1000000000.0)
        p = _softmax(scores, axis=-1)
        ctx = torch.einsum('bhqk,bhkd->bhqd', p, v)
        return torch.einsum('bhqd,hdm->bqm', ctx, w_o)

    def _B_ref(x, w_gate, w_up, w_down):
        g = torch.einsum('bsd,df->bsf', x, w_gate)
        u = torch.einsum('bsd,df->bsf', x, w_up)
        h = _sigmoid(g) * u
        return torch.einsum('bsf,fd->bsd', h, w_down)
    x = A_x
    mod = (_silu(c) @ w_mod).reshape(c.shape[0], 6, 1, -1)
    sa, ca, ga, sb, cb, gb = (mod[:, 0].clone(), mod[:, 1].clone(), mod[:, 2].clone(), mod[:, 3].clone(), mod[:, 4].clone(), mod[:, 5].clone())
    h = _layernorm(x, axis=-1) * (1 + ca) + sa
    x = x + ga * _A_ref(h, A_c_kv, A_w_q, A_w_uk, A_w_uv, A_w_o)
    h = _layernorm(x, axis=-1) * (1 + cb) + sb
    x = x + gb * _B_ref(h, B_w_gate, B_w_up, B_w_down)
    return x
```

```cpp
#include <hip/hip_runtime.h>
#include <hip/hip_bf16.h>
#include <stdint.h>

typedef unsigned short u16;
__device__ __forceinline__ float bf2f(u16 b) { return __uint_as_float(((unsigned)b) << 16); }
__device__ __forceinline__ u16 f2bf(float f) { __hip_bfloat16 h = __float2bfloat16(f); return *reinterpret_cast<u16*>(&h); }

constexpr int M = 640, H = 32, D = 128, SK = 4096, DC = 512, F = 1728;

__global__ void k_mod(const u16* c, const u16* wmod, float* mod) {
  __shared__ float s[2][M];
  __shared__ float red[4][2][64];
  int tid = threadIdx.x;
  for (int i = tid; i < 2 * M; i += 256) { float v = bf2f(c[i]); s[i / M][i % M] = v / (1.f + __expf(-v)); }
  __syncthreads();
  int n = blockIdx.x * 64 + (tid & 63), g = tid >> 6;
  float a0 = 0, a1 = 0;
  for (int k = g; k < M; k += 4) { float w = bf2f(wmod[(size_t)k * 3840 + n]); a0 += s[0][k] * w; a1 += s[1][k] * w; }
  red[g][0][tid & 63] = a0; red[g][1][tid & 63] = a1;
  __syncthreads();
  if (tid < 128) { int b = tid >> 6, cc = tid & 63; mod[b * 3840 + blockIdx.x * 64 + cc] = red[0][b][cc] + red[1][b][cc] + red[2][b][cc] + red[3][b][cc]; }
}

__device__ void ln_mod(const float* xin  , const float* mod, int sh_off, int sc_off, float (*h)[M], float* tmp) {
  int tid = threadIdx.x;
  if (tid < 128) {
    int b = tid >> 6, l = tid & 63;
    float sm = 0;
    for (int i = l; i < M; i += 64) sm += xin[b * M + i];
    for (int o = 32; o; o >>= 1) sm += __shfl_xor(sm, o);
    float mu = sm / M;
    float v = 0;
    for (int i = l; i < M; i += 64) { float d = xin[b * M + i] - mu; v += d * d; }
    for (int o = 32; o; o >>= 1) v += __shfl_xor(v, o);
    if (l == 0) { tmp[b * 2] = mu; tmp[b * 2 + 1] = 1.f / sqrtf(v / M + 1e-5f); }
  }
  __syncthreads();
  for (int i = tid; i < 2 * M; i += blockDim.x) {
    int b = i / M, m = i % M;
    h[b][m] = (xin[i] - tmp[b * 2]) * tmp[b * 2 + 1] * (1.f + mod[b * 3840 + sc_off + m]) + mod[b * 3840 + sh_off + m];
  }
  __syncthreads();
}

__global__ void k_q(const u16* x, const float* mod, const u16* wq, float* q) {
  __shared__ float xs[2 * M];
  __shared__ float h[2][M];
  __shared__ float tmp[4];
  __shared__ float red[4][2][64];
  int tid = threadIdx.x;
  for (int i = tid; i < 2 * M; i += 256) xs[i] = bf2f(x[i]);
  __syncthreads();
  ln_mod(xs, mod, 0, M, h, tmp);
  int n = blockIdx.x * 64 + (tid & 63), g = tid >> 6;
  float a0 = 0, a1 = 0;
  for (int k = g; k < M; k += 4) { float w = bf2f(wq[(size_t)k * 4096 + n]); a0 += h[0][k] * w; a1 += h[1][k] * w; }
  red[g][0][tid & 63] = a0; red[g][1][tid & 63] = a1;
  __syncthreads();
  if (tid < 128) { int b = tid >> 6, cc = tid & 63; q[b * 4096 + blockIdx.x * 64 + cc] = red[0][b][cc] + red[1][b][cc] + red[2][b][cc] + red[3][b][cc]; }
}

__global__ void k_qabs(const float* q, const u16* wuk, float* qabs) {
  int wv = (blockIdx.x * 256 + threadIdx.x) >> 6, l = threadIdx.x & 63;
  int c = wv / H, h = wv % H;
  unsigned w2 = *(const unsigned*)(wuk + ((size_t)c * H + h) * D + 2 * l);
  float w0 = bf2f(w2 & 0xffff), w1 = bf2f(w2 >> 16);
  float a0 = q[h * D + 2 * l] * w0 + q[h * D + 2 * l + 1] * w1;
  float a1 = q[4096 + h * D + 2 * l] * w0 + q[4096 + h * D + 2 * l + 1] * w1;
  for (int o = 32; o; o >>= 1) { a0 += __shfl_xor(a0, o); a1 += __shfl_xor(a1, o); }
  if (l == 0) { qabs[(0 * H + h) * DC + c] = a0 * 0.08838834764831845f; qabs[(1 * H + h) * DC + c] = a1 * 0.08838834764831845f; }
}

__global__ void k_scores(const float* qabs, const u16* ckv, float* sc) {
  int wv = (blockIdx.x * 256 + threadIdx.x) >> 6, l = threadIdx.x & 63;
  int b = wv / SK, k = wv % SK;
  uint4 v = *(const uint4*)(ckv + ((size_t)b * SK + k) * DC + 8 * l);
  float f[8] = {bf2f(v.x & 0xffff), bf2f(v.x >> 16), bf2f(v.y & 0xffff), bf2f(v.y >> 16), bf2f(v.z & 0xffff), bf2f(v.z >> 16), bf2f(v.w & 0xffff), bf2f(v.w >> 16)};
  for (int h = 0; h < H; ++h) {
    const float* qa = qabs + (b * H + h) * DC + 8 * l;
    float a = 0;
    for (int j = 0; j < 8; ++j) a += qa[j] * f[j];
    for (int o = 32; o; o >>= 1) a += __shfl_xor(a, o);
    if (l == 0) sc[((size_t)b * H + h) * SK + k] = a;
  }
}

__global__ void k_pv(const float* sc, const u16* ckv, float* ctxc) {
  __shared__ float p[SK];
  __shared__ float red[256];
  int bh = blockIdx.x >> 2, cq = blockIdx.x & 3, b = bh / H, tid = threadIdx.x;
  float mx = -1e30f;
  for (int k = tid; k < SK; k += 256) { p[k] = sc[(size_t)bh * SK + k]; mx = fmaxf(mx, p[k]); }
  red[tid] = mx; __syncthreads();
  for (int o = 128; o; o >>= 1) { if (tid < o) red[tid] = fmaxf(red[tid], red[tid + o]); __syncthreads(); }
  mx = red[0]; __syncthreads();
  float sm = 0;
  for (int k = tid; k < SK; k += 256) { p[k] = __expf(p[k] - mx); sm += p[k]; }
  red[tid] = sm; __syncthreads();
  for (int o = 128; o; o >>= 1) { if (tid < o) red[tid] += red[tid + o]; __syncthreads(); }
  sm = red[0]; __syncthreads();
  int c = cq * 128 + (tid & 127), g = tid >> 7;
  float a = 0;
  for (int k = g; k < SK; k += 2) a += p[k] * bf2f(ckv[((size_t)b * SK + k) * DC + c]);
  red[tid] = a; __syncthreads();
  if (tid < 128) ctxc[bh * DC + c] = (red[tid] + red[tid + 128]) / sm;
}

__global__ void k_ctx(const float* ctxc, const u16* wuv, float* ctx) {
  __shared__ float red[2][256];
  int h = blockIdx.x, tid = threadIdx.x, d = tid & 127, g = tid >> 7;
  float a0 = 0, a1 = 0;
  for (int c = g; c < DC; c += 2) { float w = bf2f(wuv[((size_t)c * H + h) * D + d]); a0 += ctxc[h * DC + c] * w; a1 += ctxc[(H + h) * DC + c] * w; }
  red[0][tid] = a0; red[1][tid] = a1; __syncthreads();
  if (tid < 128) { ctx[h * D + d] = red[0][tid] + red[0][tid + 128]; ctx[4096 + h * D + d] = red[1][tid] + red[1][tid + 128]; }
}

__global__ void k_o(const float* ctx, const u16* wo, const u16* x, const float* mod, float* x1) {
  __shared__ float red[2][256];
  int tid = threadIdx.x, n = blockIdx.x * 16 + (tid & 15), g = tid >> 4;
  float a0 = 0, a1 = 0;
  for (int k = g; k < 4096; k += 16) { float w = bf2f(wo[(size_t)k * M + n]); a0 += ctx[k] * w; a1 += ctx[4096 + k] * w; }
  red[0][tid] = a0; red[1][tid] = a1; __syncthreads();
  if (tid < 32) { int b = tid >> 4, cc = tid & 15; float s = 0; for (int j = 0; j < 16; ++j) s += red[b][j * 16 + cc];
    int nn = blockIdx.x * 16 + cc; x1[b * M + nn] = bf2f(x[b * M + nn]) + mod[b * 3840 + 2 * M + nn] * s; }
}

__global__ void k_gu(const float* x1, const float* mod, const u16* wg, const u16* wu, float* hh) {
  __shared__ float xs[2 * M];
  __shared__ float h[2][M];
  __shared__ float tmp[4];
  __shared__ float red[4][256];
  int tid = threadIdx.x;
  for (int i = tid; i < 2 * M; i += 256) xs[i] = x1[i];
  __syncthreads();
  ln_mod(xs, mod, 3 * M, 4 * M, h, tmp);
  int n = blockIdx.x * 16 + (tid & 15), g = tid >> 4;
  float g0 = 0, g1 = 0, u0 = 0, u1 = 0;
  for (int k = g; k < M; k += 16) { float a = bf2f(wg[(size_t)k * F + n]), b = bf2f(wu[(size_t)k * F + n]); g0 += h[0][k] * a; g1 += h[1][k] * a; u0 += h[0][k] * b; u1 += h[1][k] * b; }
  red[0][tid] = g0; red[1][tid] = g1; red[2][tid] = u0; red[3][tid] = u1; __syncthreads();
  if (tid < 32) { int b = tid >> 4, cc = tid & 15; float sg = 0, su = 0; for (int j = 0; j < 16; ++j) { sg += red[b][j * 16 + cc]; su += red[2 + b][j * 16 + cc]; }
    hh[b * F + blockIdx.x * 16 + cc] = su / (1.f + __expf(-sg)); }
}

__global__ void k_down(const float* hh, const u16* wd, const float* x1, const float* mod, u16* out) {
  __shared__ float red[2][256];
  int tid = threadIdx.x, n = blockIdx.x * 16 + (tid & 15), g = tid >> 4;
  float a0 = 0, a1 = 0;
  for (int k = g; k < F; k += 16) { float w = bf2f(wd[(size_t)k * M + n]); a0 += hh[k] * w; a1 += hh[F + k] * w; }
  red[0][tid] = a0; red[1][tid] = a1; __syncthreads();
  if (tid < 32) { int b = tid >> 4, cc = tid & 15; float s = 0; for (int j = 0; j < 16; ++j) s += red[b][j * 16 + cc];
    int nn = blockIdx.x * 16 + cc; out[b * M + nn] = f2bf(x1[b * M + nn] + mod[b * 3840 + 5 * M + nn] * s); }
}

extern "C" void kernel_launch(void* const* d_in, const int* in_sizes, int n_in,
                              void* d_out, int out_size, void* d_ws, size_t ws_size,
                              hipStream_t stream) {
  const u16* x = (const u16*)d_in[0];
  const u16* ckv = (const u16*)d_in[1];
  const u16* wq = (const u16*)d_in[2];
  const u16* wuk = (const u16*)d_in[3];
  const u16* wuv = (const u16*)d_in[4];
  const u16* wo = (const u16*)d_in[5];
  const u16* wg = (const u16*)d_in[7];
  const u16* wu = (const u16*)d_in[8];
  const u16* wd = (const u16*)d_in[9];
  const u16* c = (const u16*)d_in[10];
  const u16* wmod = (const u16*)d_in[11];
  float* ws = (float*)d_ws;
  float* mod = ws;
  float* q = ws + 8192;
  float* qabs = ws + 16384;
  float* ctxc = ws + 49152;
  float* ctx = ws + 81920;
  float* x1 = ws + 90112;
  float* hh = ws + 92160;
  float* sc = ws + 98304;
  k_mod<<<60, 256, 0, stream>>>(c, wmod, mod);
  k_q<<<64, 256, 0, stream>>>(x, mod, wq, q);
  k_qabs<<<DC * H / 4, 256, 0, stream>>>(q, wuk, qabs);
  k_scores<<<2 * SK / 4, 256, 0, stream>>>(qabs, ckv, sc);
  k_pv<<<256, 256, 0, stream>>>(sc, ckv, ctxc);
  k_ctx<<<32, 256, 0, stream>>>(ctxc, wuv, ctx);
  k_o<<<40, 256, 0, stream>>>(ctx, wo, x, mod, x1);
  k_gu<<<108, 256, 0, stream>>>(x1, mod, wg, wu, hh);
  k_down<<<40, 256, 0, stream>>>(hh, wd, x1, mod, (u16*)d_out);
}
```

```cpp
#include <hip/hip_runtime.h>
#include <hip/hip_bf16.h>
#include <stdint.h>

typedef unsigned short u16;
typedef unsigned long long u64;
typedef unsigned v4u __attribute__((ext_vector_type(4)));
typedef unsigned v2u __attribute__((ext_vector_type(2)));
typedef float f32x4 __attribute__((ext_vector_type(4)));
typedef float f32x2 __attribute__((ext_vector_type(2)));
typedef short s16x4 __attribute__((ext_vector_type(4)));
typedef __bf16 bf16x8_t __attribute__((ext_vector_type(8)));
typedef __bf16 bf16x2_t __attribute__((ext_vector_type(2)));
#define GAS __attribute__((address_space(1)))
#define LAS __attribute__((address_space(3)))
#define DI __device__ __forceinline__
#define RLX_AGENT __ATOMIC_RELAXED, __HIP_MEMORY_SCOPE_AGENT

constexpr int WS_EPOCH = 0;
constexpr int WS_TOK   = 4096;
constexpr int WS_HINT2 = 16384;
constexpr int WS_HINT3 = 32768;
constexpr int WS_HINT5 = 49152;
constexpr int WS_H1    = 65536;
constexpr int WS_MR    = WS_H1 + 20480;
constexpr int WS_H3S   = WS_MR + 20480;
constexpr int WS_H4    = WS_H3S + 32768;
constexpr int WS_R1    = WS_H4 + 65536;
constexpr int WS_H2    = WS_R1;
constexpr int WS_H5    = WS_H2 + 131072;
constexpr int WS_H6    = WS_R1 + 163840;
constexpr int WS_H3    = 1 << 20;
constexpr int WS_BYTES = WS_H3 + (2 << 20);
constexpr int WS_STAMP = 4 << 20;
constexpr int NSTAMP = 20;
static_assert(WS_H6 + 276480 <= WS_H3, "ws map");

constexpr int CKV_STR = 1104;
constexpr int L_TILE = 0;
constexpr int L_TILEB = 81920;
constexpr int L_Q = 81920;
constexpr int L_S = L_Q + 16 * 1040;
constexpr int L_P = L_S + 2 * 16 * 528;
constexpr int L_MISC = 141312;
constexpr int L_STAMP = 163840 - 256;
constexpr int LDS_BYTES = 163840;
static_assert(128 * CKV_STR <= L_MISC && L_P + 16 * 272 <= L_MISC && L_TILEB + 32768 <= L_MISC, "lds map");

#define DLY_P0 0
#define DLY_A0 16
#define DLY_2 0
#define DLY_3 8
#define DLY_4 14
#define DLY_5 20
#define DLY_6 14
constexpr int SPIN_CAP = 1 << 18;
constexpr unsigned SENT = 0xFFFFFFFFu;

struct Params {
  const u16 *x, *ckv, *wq, *wuk, *wuv, *wo, *wg, *wu, *wd, *c, *wmod;
  u16* out;
  unsigned char* ws;
};

DI float asf(unsigned u) { return __builtin_bit_cast(float, u); }
DI unsigned asu(float f) { return __builtin_bit_cast(unsigned, f); }
DI unsigned pk2(float a, float b) { f32x2 v = {a, b}; return __builtin_bit_cast(unsigned, __builtin_convertvector(v, bf16x2_t)); }
DI float blo(unsigned w) { return asf(w << 16); }
DI float bhi(unsigned w) { return asf(w & 0xFFFF0000u); }
DI f32x4 mfma(v4u a, v4u b, f32x4 c) { return __builtin_amdgcn_mfma_f32_16x16x32_bf16(__builtin_bit_cast(bf16x8_t, a), __builtin_bit_cast(bf16x8_t, b), c, 0, 0, 0); }
template <int CTRL> DI unsigned dppu(unsigned v) { return (unsigned)__builtin_amdgcn_update_dpp(0, (int)v, CTRL, 0xf, 0xf, false); }
template <int CTRL> DI float dppf(float v) { return asf(dppu<CTRL>(asu(v))); }
constexpr int DPP_X1 = 0xB1, DPP_X2 = 0x4E, DPP_HMIR = 0x141, DPP_MIR = 0x140, DPP_ROR8 = 0x128;
DI float sum4l(float v) { v += dppf<DPP_X1>(v); v += dppf<DPP_X2>(v); return v; }
DI float sum8l(float v) { v = sum4l(v); v += dppf<DPP_HMIR>(v); return v; }
DI float sum16l(float v) { v = sum8l(v); v += dppf<DPP_MIR>(v); return v; }
DI float max16l(float v) { v = fmaxf(v, dppf<DPP_X1>(v)); v = fmaxf(v, dppf<DPP_X2>(v)); v = fmaxf(v, dppf<DPP_HMIR>(v)); v = fmaxf(v, dppf<DPP_MIR>(v)); return v; }
DI float addx16(float v) { auto s = __builtin_amdgcn_permlane16_swap(asu(v), asu(v), false, false); return asf(s[0]) + asf(s[1]); }
DI float addx32(float v) { auto s = __builtin_amdgcn_permlane32_swap(asu(v), asu(v), false, false); return asf(s[0]) + asf(s[1]); }
DI float maxx16(float v) { auto s = __builtin_amdgcn_permlane16_swap(asu(v), asu(v), false, false); return fmaxf(asf(s[0]), asf(s[1])); }
DI float maxx32(float v) { auto s = __builtin_amdgcn_permlane32_swap(asu(v), asu(v), false, false); return fmaxf(asf(s[0]), asf(s[1])); }
DI float ex2(float v) { return __builtin_amdgcn_exp2f(v); }
DI float rcp(float v) { return __builtin_amdgcn_rcpf(v); }
DI float sigm(float v) { return rcp(1.f + ex2(-1.4426950408889634f * v)); }
#define WAIT_VM0() asm volatile("s_waitcnt vmcnt(0)" ::: "memory")
#define WAIT_LGKM0() asm volatile("s_waitcnt lgkmcnt(0)" ::: "memory")
#define BAR() asm volatile("s_waitcnt lgkmcnt(0)\n\ts_barrier" ::: "memory")
#define CFENCE() asm volatile("" ::: "memory")
template <int N> DI void nap() {
#pragma unroll
  for (int i = 0; i < N / 64; ++i) __builtin_amdgcn_s_sleep(64);
  if (N % 64) __builtin_amdgcn_s_sleep(N % 64);
}
DI void dma16(const void* base, unsigned voff, unsigned ldsaddr) {
  unsigned keep;
  asm volatile("s_mov_b32 %0, m0\n\ts_mov_b32 m0, %3\n\ts_nop 0\n\tglobal_load_lds_dwordx4 %1, %2 nt\n\ts_mov_b32 m0, %0"
               : "=&s"(keep) : "v"(voff), "s"(base), "s"(ldsaddr) : "memory");
}
DI void dma16v(const void* lane_ptr, unsigned ldsaddr) {
  unsigned keep;
  asm volatile("s_mov_b32 %0, m0\n\ts_mov_b32 m0, %2\n\ts_nop 0\n\tglobal_load_lds_dwordx4 %1, off nt\n\ts_mov_b32 m0, %0"
               : "=&s"(keep) : "v"(lane_ptr), "s"(ldsaddr) : "memory");
}
typedef __amdgpu_buffer_rsrc_t rsrc_t;
DI v4u ld16(rsrc_t rs, int off) { return __builtin_amdgcn_raw_buffer_load_b128(rs, off, 0, 16); }
DI v2u ld8(rsrc_t rs, int off) { return __builtin_amdgcn_raw_buffer_load_b64(rs, off, 0, 16); }
DI void st16(rsrc_t rs, int off, v4u v) { __builtin_amdgcn_raw_buffer_store_b128(v, rs, off, 0, 16); }
DI void st8(rsrc_t rs, int off, v2u v) { __builtin_amdgcn_raw_buffer_store_b64(v, rs, off, 0, 16); }
DI v4u ldnt16(const void* p) { return __builtin_nontemporal_load((const GAS v4u*)p); }
DI unsigned umin_(unsigned a, unsigned b) { return a < b ? a : b; }
DI unsigned umax_(unsigned a, unsigned b) { return a > b ? a : b; }
DI unsigned nosent(unsigned w) { return umin_(w, 0xFFFFFFFEu); }
DI bool ok_sent(v4u v) { return umax_(umax_(v.x, v.y), umax_(v.z, v.w)) != SENT; }
DI bool ok_tag(v4u v, unsigned tb) { return ((((v.x ^ tb) | (v.y ^ tb)) | ((v.z ^ tb) | (v.w ^ tb))) & 0xFFu) == 0u; }
DI unsigned tagf(float f, unsigned tb) { return (asu(f) & 0xFFFFFF00u) | tb; }
DI v4u tag4(f32x4 f, unsigned tb) { return v4u{tagf(f.x, tb), tagf(f.y, tb), tagf(f.z, tb), tagf(f.w, tb)}; }
DI f32x4 untag4(v4u v) { return f32x4{asf(v.x & 0xFFFFFF00u), asf(v.y & 0xFFFFFF00u), asf(v.z & 0xFFFFFF00u), asf(v.w & 0xFFFFFF00u)}; }

template <int MODE> DI bool okv(v4u v, unsigned t) {
  if (MODE == 0) return ok_sent(v);
  if (MODE == 1) return ok_tag(v, t);
  return v.y == t && v.w == t;
}
template <int N, int MODE0, int NB = 0, int MODE1 = 0>
DI void gather(rsrc_t rs, const int (&off)[N + NB], v4u (&v)[N + NB], unsigned t0, unsigned t1 = 0) {
  CFENCE();
#pragma unroll
  for (int i = 0; i < N + NB; ++i) v[i] = ld16(rs, off[i]);
  for (int spins = 0; spins < SPIN_CAP; ++spins) {
    bool ok = true;
#pragma unroll
    for (int i = 0; i < N + NB; ++i) ok &= (i < N) ? okv<MODE0>(v[i], t0) : okv<MODE1>(v[i], t1);
    if (__all(ok)) break;
    CFENCE();
#pragma unroll
    for (int i = 0; i < N + NB; ++i) {
      const bool oki = (i < N) ? okv<MODE0>(v[i], t0) : okv<MODE1>(v[i], t1);
      if (!oki) v[i] = ld16(rs, off[i]);
    }
  }
  CFENCE();
}
DI void wait_tag64(rsrc_t rs, int off, unsigned tlo, unsigned thi) {
  for (int spins = 0; spins < SPIN_CAP; ++spins) {
    CFENCE();
    const v2u v = ld8(rs, off);
    if (__all(v.x == tlo && v.y == thi)) break;
    __builtin_amdgcn_s_sleep(1);
  }
  CFENCE();
}
DI v4u trfrag(LAS unsigned char* L, unsigned a0, unsigned a1) {
  const s16x4 x = __builtin_amdgcn_ds_read_tr16_b64_v4i16((LAS s16x4*)(L + a0));
  const s16x4 y = __builtin_amdgcn_ds_read_tr16_b64_v4i16((LAS s16x4*)(L + a1));
  const v2u xx = __builtin_bit_cast(v2u, x), yy = __builtin_bit_cast(v2u, y);
  return v4u{xx.x, xx.y, yy.x, yy.y};
}
DI v4u diagfrag(unsigned bits16, int lane) {
  const unsigned v = bits16 << (16 * (lane & 1));
  const int d = (lane & 7) >> 1;
  return v4u{d == 0 ? v : 0u, d == 1 ? v : 0u, d == 2 ? v : 0u, d == 3 ? v : 0u};
}

#define STAMP(k) do { } while (0)

DI unsigned tile_f(unsigned row) { return ((row >> 1) & 1u) | (((row >> 3) & 1u) << 1); }
template <int NI, bool TWO = false>
DI void tile_dma(const void* baseL, const void* baseR, unsigned stride, int k0, int lane, unsigned ldsbase, int rot) {
  const unsigned r8 = lane >> 3, cpos = lane & 7;
#pragma unroll
  for (int i = 0; i < NI; ++i) {
    int ii = i + rot; if (ii >= NI) ii -= NI;
    const unsigned row = k0 + 8 * ii + r8;
    const unsigned ch = ((((cpos >> 1) ^ tile_f(row)) << 1) | (cpos & 1));
    if (TWO) {
      const char* bp = (const char*)((ch >> 2) ? baseR : baseL);
      dma16v(bp + (row * stride + (ch & 3) * 16), ldsbase + (unsigned)(k0 + 8 * ii) * 128u);
    } else {
      dma16(baseL, row * stride + ch * 16, ldsbase + (unsigned)(k0 + 8 * ii) * 128u);
    }
  }
}
template <int NS>
DI void tile_frags(LAS unsigned char* L, unsigned ldsoff, int k0, int lane, v4u (&w)[NS][4]) {
  const unsigned g = lane >> 4, q = (lane & 15) >> 2, p = lane & 3;
  const unsigned f = (q >> 1) | ((g & 1) << 1);
  const unsigned base = ldsoff + (k0 + 8 * g + q) * 128 + 8 * p;
#pragma unroll
  for (int s = 0; s < NS; ++s)
#pragma unroll
    for (int nt = 0; nt < 4; ++nt) {
      const unsigned a = base + s * 4096 + ((nt ^ f) * 32);
      w[s][nt] = trfrag(L, a, a + 512);
    }
}

__global__ void __launch_bounds__(512, 2) mk_kernel(Params P) {
  __shared__ __attribute__((aligned(16))) unsigned char smem[LDS_BYTES];
  LAS unsigned char* L = (LAS unsigned char*)smem;
  const unsigned Lb = (unsigned)(size_t)L;
  const int tid = threadIdx.x, lane = tid & 63;
  const int w = __builtin_amdgcn_readfirstlane(tid >> 6);
  const int bid = blockIdx.x, xc = bid & 7, r = bid >> 3;
  const int g = lane >> 4, i16 = lane & 15;
  STAMP(0);
  const rsrc_t rs = __builtin_amdgcn_make_buffer_rsrc((void*)P.ws, 0, WS_BYTES, 0x00020000);
  const v2u ep = ld8(rs, WS_EPOCH);
  const v4u SENT4 = v4u{SENT, SENT, SENT, SENT};
  const int k0w = w < 4 ? 96 * w : 384 + 64 * (w - 4);

  if (r < 16) {
    const int h = (r >> 2) * 8 + xc, sub = r & 3, dh = sub >> 1, ch = sub & 1, hg = r >> 3;
    const int pi = (r & 7) * 8 + xc, pidx = r * 8 + xc;
    nap<DLY_P0>();
    {
      const u16* wq = P.wq + h * 128 + dh * 64;
      if (w < 4) tile_dma<12>(wq, wq + 32, 8192, k0w, lane, Lb + L_TILE, ch * 6);
      else       tile_dma<8>(wq, wq + 32, 8192, k0w, lane, Lb + L_TILE, ch * 4);
      const u16* wk = P.wuk + h * 128 + dh * 64;
#pragma unroll
      for (int i = 0; i < 4; ++i) {
        const unsigned lr = 8 * i + (lane >> 3);
        const unsigned sw = ((lr >> 1) & 1u) | (((lr >> 3) & 3u) << 1);
        const unsigned voff = (256 * ch + 32 * w + lr) * 8192u + (((lane & 7) ^ sw) * 16);
        dma16(wk, voff, Lb + L_TILEB + w * 4096 + i * 1024);
      }
      const int c3 = WS_H3 + (h * 4 + sub) * 16384 + tid * 16;
      st16(rs, c3, SENT4); st16(rs, c3 + 8192, SENT4);
      if (tid < 32) st16(rs, WS_H4 + (h * 4 + sub) * 512 + tid * 16, SENT4);
    }
    WAIT_VM0();
    STAMP(1);
    const unsigned elo = __builtin_amdgcn_readfirstlane(ep.x), ehi = __builtin_amdgcn_readfirstlane(ep.y);
    const unsigned tlo = elo + 1u, thi = ehi + (tlo == 0u ? 1u : 0u), tb = tlo & 0xFFu;
    v4u wf[3][4];
    v4u uk[2][2];
    if (w < 4) tile_frags<3>(L, L_TILE, k0w, lane, wf);
    else { v4u (&w2)[2][4] = reinterpret_cast<v4u (&)[2][4]>(wf); tile_frags<2>(L, L_TILE, k0w, lane, w2);
#pragma unroll
      for (int nt = 0; nt < 4; ++nt) wf[2][nt] = v4u{0, 0, 0, 0}; }
#pragma unroll
    for (int u = 0; u < 2; ++u) {
      const unsigned lr = 8 * (i16 >> 2) + 4 * u + (i16 & 3);
      const unsigned sw = ((lr >> 1) & 1u) | (((lr >> 3) & 3u) << 1);
#pragma unroll
      for (int t = 0; t < 2; ++t) uk[u][t] = *(LAS v4u*)(L + L_TILEB + w * 4096 + lr * 128 + (((4 * t + g) ^ sw) * 16));
    }
    WAIT_LGKM0();
    STAMP(2);
    {
      const int ts = i16 >> 3, b = (i16 >> 2) & 1, p = i16 & 3;
      const int o = WS_H1 + ((p * 2 + b) * 640 + k0w) * 4 + (2 * g + ts) * 16;
      int off[3] = {o, o + 128, w < 4 ? o + 256 : o};
      v4u v[3];
      gather<3, 1>(rs, off, v, tb);
      if (tid == 0) st8(rs, WS_HINT2 + pidx * 128, v2u{tlo, thi});
      STAMP(3);
      unsigned pk[3][2];
#pragma unroll
      for (int j = 0; j < 3; ++j) { pk[j][0] = pk2(asf(v[j].x), asf(v[j].y)); pk[j][1] = pk2(asf(v[j].z), asf(v[j].w)); }
      const unsigned s0 = ts ? pk[0][0] : pk[1][0], s1 = ts ? pk[0][1] : pk[1][1];
      const unsigned r0 = dppu<DPP_ROR8>(s0), r1 = dppu<DPP_ROR8>(s1);
      const v4u fab = ts ? v4u{r0, r1, pk[1][0], pk[1][1]} : v4u{pk[0][0], pk[0][1], r0, r1};
      const unsigned q0 = dppu<DPP_ROR8>(pk[2][0]), q1 = dppu<DPP_ROR8>(pk[2][1]);
      const v4u fc = ts ? v4u{q0, q1, pk[2][0], pk[2][1]} : v4u{pk[2][0], pk[2][1], q0, q1};
      f32x4 a0[4], a1[4];
      const f32x4 z = {0.f, 0.f, 0.f, 0.f};
#pragma unroll
      for (int nt = 0; nt < 4; ++nt) { a0[nt] = mfma(fab, wf[0][nt], z); a1[nt] = mfma(fab, wf[1][nt], z); }
      if (w < 4) {
#pragma unroll
        for (int nt = 0; nt < 4; ++nt) a0[nt] = mfma(fc, wf[2][nt], a0[nt]);
      }
      float qv[4];
#pragma unroll
      for (int nt = 0; nt < 4; ++nt) {
        const f32x4 a = g < 2 ? a0[nt] : a1[nt];
        qv[nt] = addx32((a.x + a.y) + (a.z + a.w));
      }
      const float e0 = g < 2 ? qv[0] : qv[2], e1 = g < 2 ? qv[1] : qv[3];
      const unsigned pe = pk2(e0, e1);
      LAS u16* qx = (LAS u16*)(L + L_MISC + (8 * (g & 1) + w) * 144) + (g >> 1) * 32 + i16;
      qx[0] = (u16)(pe & 0xFFFFu); qx[16] = (u16)(pe >> 16);
    }
    BAR();
    {
      const v4u qb0 = *(LAS v4u*)(L + L_MISC + i16 * 144 + g * 16);
      const v4u qb1 = *(LAS v4u*)(L + L_MISC + i16 * 144 + 64 + g * 16);
      const f32x4 z = {0.f, 0.f, 0.f, 0.f};
      f32x4 d0 = mfma(uk[0][0], qb0, z), d1 = mfma(uk[1][0], qb0, z);
      d0 = mfma(uk[0][1], qb1, d0); d1 = mfma(uk[1][1], qb1, d1);
      const float sc = 0.08838834764831845f * 1.4426950408889634f;
      float e[8];
#pragma unroll
      for (int j = 0; j < 4; ++j) { e[j] = sum8l(d0[j]) * sc; e[4 + j] = sum8l(d1[j]) * sc; }
      const v4u o = v4u{nosent(pk2(e[0], e[1])), nosent(pk2(e[2], e[3])), nosent(pk2(e[4], e[5])), nosent(pk2(e[6], e[7]))};
      if ((i16 & 7) == 0) st16(rs, WS_H2 + ((((i16 >> 3) * 32 + h) * 2 + dh) * 512 + 256 * ch + 32 * w + 8 * g) * 2, o);
    }
    STAMP(4);
    const bool roleO = pi < 40, roleM = pi >= 40 && pi < 60;
    const int kr = hg * 4 + pi / 10, ng = pi % 10, lc = hg * 20 + (pi - 40);
    {
      const u16* wv = P.wuv + (size_t)(128 * sub) * 4096 + h * 128;
      tile_dma<2>(wv, wv + 32, 8192, 16 * w, lane, Lb + L_TILEB, 0);
      tile_dma<2>(wv + 64, wv + 96, 8192, 16 * w, lane, Lb + L_TILEB + 16384, 0);
      if (roleO) {
        const u16* wo = P.wo + (size_t)(512 * kr) * 640 + 64 * ng;
        tile_dma<8>(wo, wo + 32, 1280, 64 * w, lane, Lb + L_TILE, 0);
      } else if (roleM) {
        const u16* wm = P.wmod + 1280 + 64 * lc;
        if (w < 4) tile_dma<12>(wm, wm + 32, 7680, k0w, lane, Lb + L_TILE, 0);
        else       tile_dma<8>(wm, wm + 32, 7680, k0w, lane, Lb + L_TILE, 0);
      }
    }
    v4u cs[3];
    if (roleM) {
#pragma unroll
      for (int s = 0; s < 3; ++s) cs[s] = *(const GAS v4u*)(P.c + (i16 & 1) * 640 + k0w + ((w < 4 || s < 2) ? 32 * s : 0) + 8 * g);
    }
    WAIT_VM0();
    BAR();
    STAMP(5);
    v4u uv[4];
    {
      const unsigned q = i16 >> 2, p = lane & 3, f = (q >> 1) | ((g & 1) << 1);
      const unsigned base = L_TILEB + (w >> 2) * 16384 + (8 * g + q) * 128 + 8 * p + (((w & 3) ^ f) * 32);
#pragma unroll
      for (int t = 0; t < 4; ++t) uv[t] = trfrag(L, base + t * 4096, base + t * 4096 + 512);
    }
    f32x4 gav = {0.f, 0.f, 0.f, 0.f};
    if (roleO) {
      v4u (&w2)[2][4] = reinterpret_cast<v4u (&)[2][4]>(wf);
      tile_frags<2>(L, L_TILE, 64 * w, lane, w2);
    } else if (roleM) {
      if (w < 4) tile_frags<3>(L, L_TILE, k0w, lane, wf);
      else { v4u (&w2)[2][4] = reinterpret_cast<v4u (&)[2][4]>(wf); tile_frags<2>(L, L_TILE, k0w, lane, w2); }
      f32x4 acc[4];
#pragma unroll
      for (int nt = 0; nt < 4; ++nt) acc[nt] = f32x4{0.f, 0.f, 0.f, 0.f};
#pragma unroll
      for (int s = 0; s < 3; ++s) {
        if (s < 2 || w < 4) {
          v4u a;
#pragma unroll
          for (int j = 0; j < 4; ++j) { const float lo = blo(cs[s][j]), hi = bhi(cs[s][j]); a[j] = pk2(lo * sigm(lo), hi * sigm(hi)); }
#pragma unroll
          for (int nt = 0; nt < 4; ++nt) acc[nt] = mfma(a, wf[s][nt], acc[nt]);
        }
      }
      const f32x4 a = g == 0 ? acc[0] : g == 1 ? acc[1] : g == 2 ? acc[2] : acc[3];
      LAS float* red = (LAS float*)(L + L_MISC + 4096);
      red[(w * 2 + 0) * 64 + lane] = a.x; red[(w * 2 + 1) * 64 + lane] = a.y;
      BAR();
      if (tid < 32) {
        f32x4 s = {0.f, 0.f, 0.f, 0.f};
#pragma unroll
        for (int ww = 0; ww < 8; ++ww) s += *(LAS f32x4*)(red + (ww * 2 + (tid >> 4)) * 64 + (tid & 15) * 4);
        st16(rs, WS_MR + ((tid >> 4) * 2560 + 64 * lc + (tid & 15) * 4) * 4, tag4(s, tb));
      }
    }
    WAIT_LGKM0();
    STAMP(6);
    if (roleO && w == 0) {
      int off[1] = {WS_MR + (((lane >> 4) & 1) * 2560 + 64 * ng + i16 * 4) * 4};
      v4u v[1];
      gather<1, 1>(rs, off, v, tb);
      gav = untag4(v[0]);
    }
    STAMP(7);
    wait_tag64(rs, WS_HINT3 + (((pi & 31) * 4 + (lane & 1) * 2 + hg) & 127) * 128, tlo, thi);
    STAMP(8);
    nap<DLY_3>();
    {
      const int b = w >> 2, sq = w & 3;
      const int od = WS_H3 + (h * 4 + sub) * 16384 + (b * 32 + 8 * sq) * 256 + lane * 16;
      const int os = WS_H3S + (((b * 2 + hg) * 32 + 8 * sq + g) * 16 + (h & 15)) * 16;
      int off[4] = {od, od + 1024, os, os + 1024};
      v4u v[4];
      gather<2, 0, 2, 2>(rs, off, v, 0u, tlo);
      STAMP(9);
      const float m0 = asf(v[2].x), l0 = asf(v[2].z), m1 = asf(v[3].x), l1 = asf(v[3].z);
      const float mw = maxx32(maxx16(fmaxf(m0, m1)));
      const float e0 = ex2(m0 - mw), e1 = ex2(m1 - mw);
      const float lw = addx32(addx16(e0 * l0 + e1 * l1));
      const unsigned ph = pk2(e0, e1);
      const unsigned pl = pk2(e0 - blo(ph), e1 - bhi(ph));
      const unsigned pe = i16 < 8 ? ph : pl;
      const f32x4 z = {0.f, 0.f, 0.f, 0.f};
      f32x4 d = mfma(diagfrag(pe & 0xFFFFu, lane), v[0], z);
      d = mfma(diagfrag(pe >> 16, lane), v[1], d);
      float c4[4];
#pragma unroll
      for (int j = 0; j < 4; ++j) c4[j] = addx32(d[j]);
      const unsigned h0 = pk2(c4[0], c4[1]), h1 = pk2(c4[2], c4[3]);
      const unsigned o0 = pk2(c4[0] - blo(h0), c4[1] - bhi(h0)), o1 = pk2(c4[2] - blo(h1), c4[3] - bhi(h1));
      *(LAS v2u*)(L + L_MISC + 8192 + (2 * w + (g >> 1)) * 272 + (8 * i16 + 4 * (g & 1)) * 2) = g < 2 ? v2u{h0, h1} : v2u{o0, o1};
      if (lane == 0) *(LAS f32x2*)(L + L_MISC + 12800 + w * 8) = f32x2{mw, lw};
    }
    BAR();
    {
      v4u xb[4];
#pragma unroll
      for (int t = 0; t < 4; ++t) xb[t] = *(LAS v4u*)(L + L_MISC + 8192 + i16 * 272 + t * 64 + g * 16);
      const f32x4 sa = *(LAS f32x4*)(L + L_MISC + 12800 + (i16 >> 3) * 32), sb = *(LAS f32x4*)(L + L_MISC + 12800 + (i16 >> 3) * 32 + 16);
      const f32x2 me = *(LAS f32x2*)(L + L_MISC + 12800 + (i16 >> 1) * 8);
      f32x4 d = {0.f, 0.f, 0.f, 0.f};
#pragma unroll
      for (int t = 0; t < 4; ++t) d = mfma(uv[t], xb[t], d);
      const float M = fmaxf(fmaxf(sa.x, sa.z), fmaxf(sb.x, sb.z));
      const float Ls = ex2(sa.x - M) * sa.y + ex2(sa.z - M) * sa.w + ex2(sb.x - M) * sb.y + ex2(sb.z - M) * sb.w;
      const float f = ex2(me.x - M) / Ls;
      float e[4];
#pragma unroll
      for (int j = 0; j < 4; ++j) e[j] = sum8l(d[j] * f);
      if ((i16 & 7) == 0) st8(rs, WS_H4 + (((h * 4 + sub) * 2 + (i16 >> 3)) * 128 + 16 * w + 4 * g) * 2, v2u{nosent(pk2(e[0], e[1])), nosent(pk2(e[2], e[3]))});
      if (tid == 0) st8(rs, WS_HINT5 + pidx * 128, v2u{tlo, thi});
    }
    STAMP(10);
    if (roleO) {
      nap<DLY_4>();
      const int ts = i16 >> 3, b = (i16 >> 2) & 1, p = i16 & 3;
      int off[1] = {WS_H4 + ((((4 * kr + (w >> 1)) * 4 + p) * 2 + b) * 128 + 64 * (w & 1) + 32 * ts + 8 * g) * 2};
      v4u v[1];
      gather<1, 0>(rs, off, v, 0u);
      STAMP(11);
      const f32x4 z = {0.f, 0.f, 0.f, 0.f};
      LAS float* red = (LAS float*)(L + L_MISC + 13312);
#pragma unroll
      for (int nt = 0; nt < 4; ++nt) {
        const f32x4 a0 = mfma(v[0], wf[0][nt], z), a1 = mfma(v[0], wf[1][nt], z);
        const f32x4 a = g < 2 ? a0 : a1;
        red[(w * 4 + g) * 64 + 16 * nt + i16] = (a.x + a.y) + (a.z + a.w);
      }
      BAR();
      if (w == 0) {
        f32x4 s = {0.f, 0.f, 0.f, 0.f};
#pragma unroll
        for (int ww = 0; ww < 4; ++ww)
#pragma unroll
          for (int t2 = 0; t2 < 2; ++t2) s += *(LAS f32x4*)(red + (((lane >> 5) * 4 + ww) * 4 + 2 * t2 + (g & 1)) * 64 + i16 * 4);
#pragma unroll
        for (int j = 0; j < 4; ++j) s[j] = addx32(s[j]) * gav[j];
        if (lane < 32) st8(rs, WS_H5 + (((kr * 20 + 2 * ng + (i16 >> 3)) * 2 + g) * 32 + (i16 & 7) * 4) * 2, v2u{nosent(pk2(s.x, s.y)), nosent(pk2(s.z, s.w))});
      }
      STAMP(12);
    }
  } else {
    const int ra = r - 16, hg = ra & 1, tile = (ra >> 1) * 8 + xc, b = tile >> 5, s = tile & 31;
    const int aidx = tile * 2 + hg;
    const bool role7 = tile < 27, role1 = tile >= 27 && tile < 47;
    const int fg = aidx, s1 = aidx - 54;
    unsigned elo, ehi, tlo, thi, tb;
    if (role1) {
      const int mg = s1 % 10, jq = s1 / 10;
      const int j0 = 160 * jq + 20 * w + g;
      u16 cv[5]; v4u wm[5];
#pragma unroll
      for (int t = 0; t < 5; ++t) cv[t] = *(const GAS u16*)(P.c + (i16 >> 3) * 640 + j0 + 4 * t);
#pragma unroll
      for (int t = 0; t < 5; ++t) wm[t] = ldnt16(P.wmod + (size_t)(j0 + 4 * t) * 3840 + (i16 >> 3) * 640 + 64 * mg + 8 * (i16 & 7));
      v2u xv[5]; v2u xm;
      if (w == 0) {
#pragma unroll
        for (int t = 0; t < 5; ++t) xv[t] = *(const GAS v2u*)(P.x + (lane >> 5) * 640 + (lane & 31) * 20 + 4 * t);
        xm = *(const GAS v2u*)(P.x + (lane >> 5) * 640 + 64 * mg + 8 * (i16 & 7) + 4 * (g & 1));
      }
      if (w >= 4) st16(rs, WS_R1 + s1 * 4096 + (tid - 256) * 16, SENT4);
      f32x4 d = {0.f, 0.f, 0.f, 0.f};
#pragma unroll
      for (int t = 0; t < 5; ++t) {
        const float cf = asf((unsigned)cv[t] << 16);
        const unsigned sb = pk2(cf * sigm(cf), 0.f) & 0xFFFFu;
        d = mfma(diagfrag(sb, lane), wm[t], d);
      }
      *(LAS f32x4*)(L + L_MISC + (w * 64 + lane) * 16) = d;
      float xn[4];
      if (w == 0) {
        float sm = 0.f;
#pragma unroll
        for (int t = 0; t < 5; ++t) sm += (blo(xv[t].x) + bhi(xv[t].x)) + (blo(xv[t].y) + bhi(xv[t].y));
        const float mu = addx16(sum16l(sm)) * (1.f / 640.f);
        float vs = 0.f;
#pragma unroll
        for (int t = 0; t < 5; ++t) { float a = blo(xv[t].x) - mu, bq = bhi(xv[t].x) - mu, c2 = blo(xv[t].y) - mu, d2 = bhi(xv[t].y) - mu; vs += (a * a + bq * bq) + (c2 * c2 + d2 * d2); }
        const float rsd = 1.f / sqrtf(addx16(sum16l(vs)) * (1.f / 640.f) + 1e-5f);
        xn[0] = (blo(xm.x) - mu) * rsd; xn[1] = (bhi(xm.x) - mu) * rsd; xn[2] = (blo(xm.y) - mu) * rsd; xn[3] = (bhi(xm.y) - mu) * rsd;
      }
      WAIT_VM0();
      BAR();
      elo = __builtin_amdgcn_readfirstlane(ep.x); ehi = __builtin_amdgcn_readfirstlane(ep.y);
      tlo = elo + 1u; thi = ehi + (tlo == 0u ? 1u : 0u); tb = tlo & 0xFFu;
      if (w == 0) {
        f32x4 sm = {0.f, 0.f, 0.f, 0.f};
#pragma unroll
        for (int ww = 0; ww < 8; ++ww) sm += *(LAS f32x4*)(L + L_MISC + (ww * 64 + lane) * 16);
        f32x4 hp;
#pragma unroll
        for (int j = 0; j < 4; ++j) { const float ca = dppf<DPP_ROR8>(sm[j]); hp[j] = xn[j] * (ca + (jq == 0 ? 1.f : 0.f)) + sm[j]; }
        if (i16 < 8) st16(rs, WS_H1 + ((jq * 2 + (g >> 1)) * 640 + 64 * mg + 8 * i16 + 4 * (g & 1)) * 4, tag4(hp, tb));
      }
      if (tid == 64) st8(rs, WS_TOK + s1 * 128, v2u{tlo, thi});
      STAMP(1);
    } else {
      nap<DLY_A0>();
    }
    {
      const u16* ck = P.ckv + ((size_t)b * 4096 + 128 * s) * 512;
#pragma unroll
      for (int i = 0; i < 16; ++i) {
        const int row = ((w + 4 * hg) & 7) * 16 + i;
        dma16(ck, (unsigned)row * 1024u + lane * 16, Lb + L_TILE + row * CKV_STR);
      }
    }
    WAIT_VM0();
    BAR();
    STAMP(2);
    elo = __builtin_amdgcn_readfirstlane(ep.x); ehi = __builtin_amdgcn_readfirstlane(ep.y);
    tlo = elo + 1u; thi = ehi + (tlo == 0u ? 1u : 0u); tb = tlo & 0xFFu;
    v4u cq[2][8], cv[4][4];
    const int kq = w & 3, chf = w >> 2;
#pragma unroll
    for (int kt = 0; kt < 2; ++kt)
#pragma unroll
      for (int t = 0; t < 8; ++t) cq[kt][t] = *(LAS v4u*)(L + L_TILE + (32 * kq + 16 * kt + i16) * CKV_STR + (256 * chf + 32 * t + 8 * g) * 2);
    {
      const unsigned q = i16 >> 2, p = lane & 3;
      const unsigned base = L_TILE + (8 * g + q) * CKV_STR + (64 * w + 8 * p) * 2;
#pragma unroll
      for (int tt = 0; tt < 4; ++tt)
#pragma unroll
        for (int kt = 0; kt < 4; ++kt) {
          const unsigned a = base + kt * 32 * CKV_STR + (32 * (tt >> 1) + 4 * (tt & 1)) * 2;
          cv[tt][kt] = trfrag(L, a, a + 4 * CKV_STR);
        }
    }
    BAR();
    STAMP(3);
    wait_tag64(rs, WS_TOK + (lane < 40 ? lane : 0) * 128, tlo, thi);
    wait_tag64(rs, WS_HINT2 + (hg * 64 + ((tile + (lane & 1) * 29) & 63)) * 128, tlo, thi);
    STAMP(4);
    nap<DLY_2>();
    {
      const int o = WS_H2 + (((b * 32 + 16 * hg + 2 * w + (g >> 1)) * 2 + (g & 1)) * 512 + 8 * i16) * 2;
      int off[4] = {o, o + 256, o + 512, o + 768};
      v4u v[4];
      gather<4, 0>(rs, off, v, 0u);
      if (tid == 0) st8(rs, WS_HINT3 + aidx * 128, v2u{tlo, thi});
      STAMP(5);
      const v4u sel = diagfrag(((g >> 1) == (i16 >> 3)) ? 0x3F80u : 0u, lane);
      const f32x4 z = {0.f, 0.f, 0.f, 0.f};
#pragma unroll
      for (int cg = 0; cg < 4; ++cg) {
        const f32x4 d = mfma(sel, v[cg], z);
        *(LAS v2u*)(L + L_Q + (2 * w + (g >> 1)) * 1040 + (128 * cg + 8 * i16 + 4 * (g & 1)) * 2) = v2u{pk2(d.x, d.y), pk2(d.z, d.w)};
      }
    }
    BAR();
    {
      f32x4 d0 = {0.f, 0.f, 0.f, 0.f}, d1 = {0.f, 0.f, 0.f, 0.f};
      v4u qf[8];
#pragma unroll
      for (int t = 0; t < 8; ++t) qf[t] = *(LAS v4u*)(L + L_Q + i16 * 1040 + (256 * chf + 32 * t + 8 * g) * 2);
#pragma unroll
      for (int t = 0; t < 8; ++t) { d0 = mfma(cq[0][t], qf[t], d0); d1 = mfma(cq[1][t], qf[t], d1); }
      LAS unsigned char* sp = L + L_S + (chf * 16 + i16) * 528 + (32 * kq + 4 * g) * 4;
      *(LAS f32x4*)sp = d0; *(LAS f32x4*)(sp + 64) = d1;
    }
    BAR();
    STAMP(6);
    {
      const int hd = tid >> 5, k4 = tid & 31;
      const f32x4 sv = *(LAS f32x4*)(L + L_S + hd * 528 + k4 * 16) + *(LAS f32x4*)(L + L_S + (16 + hd) * 528 + k4 * 16);
      const float m = maxx16(max16l(fmaxf(fmaxf(sv.x, sv.y), fmaxf(sv.z, sv.w))));
      const float p0 = ex2(sv.x - m), p1 = ex2(sv.y - m), p2 = ex2(sv.z - m), p3 = ex2(sv.w - m);
      *(LAS v2u*)(L + L_P + hd * 272 + k4 * 8) = v2u{pk2(p0, p1), pk2(p2, p3)};
      const float l = addx16(sum16l((p0 + p1) + (p2 + p3)));
      if (k4 == 0) st16(rs, WS_H3S + (((b * 2 + hg) * 32 + s) * 16 + hd) * 16, v4u{asu(m), tlo, asu(l), tlo});
    }
    BAR();
    {
      v4u pf[4];
#pragma unroll
      for (int kt = 0; kt < 4; ++kt) pf[kt] = *(LAS v4u*)(L + L_P + i16 * 272 + kt * 64 + g * 16);
      f32x4 d[4];
#pragma unroll
      for (int tt = 0; tt < 4; ++tt) d[tt] = f32x4{0.f, 0.f, 0.f, 0.f};
#pragma unroll
      for (int kt = 0; kt < 4; ++kt)
#pragma unroll
        for (int tt = 0; tt < 4; ++tt) d[tt] = mfma(cv[tt][kt], pf[kt], d[tt]);
      const int o = WS_H3 + ((16 * hg + i16) * 4 + (w >> 1)) * 16384 + (b * 32 + s) * 256 + (64 * (w & 1) + 8 * g) * 2;
#pragma unroll
      for (int u = 0; u < 2; ++u)
        st16(rs, o + u * 64, v4u{nosent(pk2(d[2 * u].x, d[2 * u].y)), nosent(pk2(d[2 * u].z, d[2 * u].w)), nosent(pk2(d[2 * u + 1].x, d[2 * u + 1].y)), nosent(pk2(d[2 * u + 1].z, d[2 * u + 1].w))});
    }
    STAMP(7);
    if (role7) {
      {
        const u16* wg = P.wg + 32 * fg; const u16* wu = P.wu + 32 * fg;
        if (w < 4) tile_dma<12, true>(wg, wu, 3456, k0w, lane, Lb + L_TILE, hg * 6);
        else       tile_dma<8, true>(wg, wu, 3456, k0w, lane, Lb + L_TILE, hg * 4);
      }
      v4u wd[8];
      if (w < 5) {
#pragma unroll
        for (int t = 0; t < 8; ++t) wd[t] = ldnt16(P.wd + (size_t)(32 * fg + 4 * t + g) * 640 + 128 * w + 8 * i16);
      }
      const int bb = (i16 >> 2) & 1, kk = k0w + 8 * (i16 & 3) + 4 * (g & 1);
      const int nst = w < 4 ? 3 : 2;
      v2u xr[3]; v2u xv[5];
#pragma unroll
      for (int v = 0; v < 3; ++v) xr[v] = *(const GAS v2u*)(P.x + bb * 640 + kk + (v < nst ? 32 * v : 0));
#pragma unroll
      for (int t = 0; t < 5; ++t) xv[t] = *(const GAS v2u*)(P.x + (lane >> 5) * 640 + (lane & 31) * 20 + 4 * t);
      v4u mv[6];
      {
        int off[6];
#pragma unroll
        for (int v = 0; v < 3; ++v) { const int k = kk + (v < nst ? 32 * v : 0); off[v] = WS_MR + (bb * 2560 + 640 + k) * 4; off[3 + v] = WS_MR + (bb * 2560 + 1280 + k) * 4; }
        gather<6, 1>(rs, off, mv, tb);
      }
      WAIT_VM0();
      STAMP(8);
      v4u wf[3][4];
      if (w < 4) tile_frags<3>(L, L_TILE, k0w, lane, wf);
      else { v4u (&w2)[2][4] = reinterpret_cast<v4u (&)[2][4]>(wf); tile_frags<2>(L, L_TILE, k0w, lane, w2);
#pragma unroll
        for (int nt = 0; nt < 4; ++nt) wf[2][nt] = v4u{0, 0, 0, 0}; }
      float piv;
      {
        float sm = 0.f;
#pragma unroll
        for (int t = 0; t < 5; ++t) sm += (blo(xv[t].x) + bhi(xv[t].x)) + (blo(xv[t].y) + bhi(xv[t].y));
        const float mu = addx16(sum16l(sm)) * (1.f / 640.f);
        const float mu0 = asf(__builtin_amdgcn_readlane(asu(mu), 0)), mu1 = asf(__builtin_amdgcn_readlane(asu(mu), 32));
        piv = bb ? mu1 : mu0;
      }
      f32x4 xc[3], oc[3];
      LAS unsigned char* yx = L + L_MISC + w * 1664;
      LAS float* red = (LAS float*)(L + L_MISC + 13312);
      LAS float* red2 = (LAS float*)(L + L_MISC + 17408);
      LAS float* wst = (LAS float*)(L + L_MISC + 21504);
      const int hl = g >> 1;
#pragma unroll
      for (int v = 0; v < 3; ++v) {
        xc[v] = f32x4{blo(xr[v].x) - piv, bhi(xr[v].x) - piv, blo(xr[v].y) - piv, bhi(xr[v].y) - piv};
        const f32x4 cb = untag4(mv[3 + v]), sb = untag4(mv[v]);
        oc[v] = f32x4{1.f + cb.x, 1.f + cb.y, 1.f + cb.z, 1.f + cb.w};
        const f32x4 y = i16 < 8 ? oc[v] : sb;
        const unsigned h0 = pk2(y.x, y.y), h1 = pk2(y.z, y.w);
        const unsigned o0 = pk2(y.x - blo(h0), y.y - bhi(h0)), o1 = pk2(y.z - blo(h1), y.w - bhi(h1));
        *(LAS v2u*)(yx + ((i16 >> 3) * 4 + bb * 2 + hl) * 208 + (32 * v + 8 * (i16 & 3) + 4 * (g & 1)) * 2) = hl ? v2u{o0, o1} : v2u{h0, h1};
      }
      WAIT_LGKM0();
      {
        f32x4 acc[4];
#pragma unroll
        for (int nt = 0; nt < 4; ++nt) acc[nt] = f32x4{0.f, 0.f, 0.f, 0.f};
#pragma unroll
        for (int v = 0; v < 3; ++v) {
          if (v < 2 || w < 4) {
            const v4u a = *(LAS v4u*)(yx + (i16 & 7) * 208 + v * 64 + g * 16);
#pragma unroll
            for (int nt = 0; nt < 4; ++nt) acc[nt] = mfma(a, wf[v][nt], acc[nt]);
          }
        }
        const f32x4 a0 = g < 2 ? acc[0] : acc[2], a1 = g < 2 ? acc[1] : acc[3];
        LAS float* dst = (g & 1) ? red2 : red;
        *(LAS f32x2*)(dst + ((w * 4 + (g >> 1) * 2 + 0) * 16 + i16) * 2) = f32x2{a0.x + a0.y, a0.z + a0.w};
        *(LAS f32x2*)(dst + ((w * 4 + (g >> 1) * 2 + 1) * 16 + i16) * 2) = f32x2{a1.x + a1.y, a1.z + a1.w};
      }
      BAR();
      float Bg = 0.f, Bu = 0.f, Cg = 0.f, Cu = 0.f;
      const int fl = lane & 31, fb = lane >> 5;
#pragma unroll
      for (int ww = 0; ww < 8; ++ww) {
        Bg += red[((ww * 4 + (fl >> 4)) * 16 + (fl & 15)) * 2 + fb]; Bu += red[((ww * 4 + 2 + (fl >> 4)) * 16 + (fl & 15)) * 2 + fb];
        Cg += red2[((ww * 4 + (fl >> 4)) * 16 + (fl & 15)) * 2 + fb]; Cu += red2[((ww * 4 + 2 + (fl >> 4)) * 16 + (fl & 15)) * 2 + fb];
      }
      const bool role9 = fg < 40;
      const int b9 = fg / 20, n9 = (fg % 20) * 32;
      f32x4 gbv = {0.f, 0.f, 0.f, 0.f}; v2u x9 = {0u, 0u};
      if (role9 && w == 0) {
        int off[1] = {WS_MR + (b9 * 2560 + 1920 + n9 + (lane & 7) * 4) * 4};
        v4u v[1];
        gather<1, 1>(rs, off, v, tb);
        gbv = untag4(v[0]);
        x9 = *(const GAS v2u*)(P.x + b9 * 640 + n9 + (lane & 7) * 4);
      }
      BAR();
      STAMP(9);
      wait_tag64(rs, WS_HINT5 + ((fg * 2 + (lane & 1) * 67) & 127) * 128, tlo, thi);
      STAMP(10);
      nap<DLY_5>();
      {
        const int o = WS_H5 + (((2 * g + (i16 >> 3)) * 20 + (k0w >> 5)) * 128) + (i16 & 7) * 16;
        int off[3] = {o, o + 128, w < 4 ? o + 256 : o};
        v4u v[3];
        gather<3, 0>(rs, off, v, 0u);
        STAMP(11);
        const v4u sel = diagfrag(0x3F80u, lane);
        const f32x4 z = {0.f, 0.f, 0.f, 0.f};
        float s1s = 0.f;
#pragma unroll
        for (int k = 0; k < 3; ++k) {
          f32x4 d = mfma(sel, v[k], z);
#pragma unroll
          for (int j = 0; j < 4; ++j) d[j] += dppf<DPP_ROR8>(d[j]);
          xc[k] += d;
          const f32x4 y = xc[k] * oc[k];
          const unsigned h0 = pk2(y.x, y.y), h1 = pk2(y.z, y.w);
          const unsigned o0 = pk2(y.x - blo(h0), y.y - bhi(h0)), o1 = pk2(y.z - blo(h1), y.w - bhi(h1));
          if (i16 < 8) *(LAS v2u*)(yx + (bb * 2 + hl) * 208 + (32 * k + 8 * (i16 & 3) + 4 * (g & 1)) * 2) = hl ? v2u{o0, o1} : v2u{h0, h1};
          if (k < 2 || w < 4) s1s += (xc[k].x + xc[k].y) + (xc[k].z + xc[k].w);
        }
        WAIT_LGKM0();
        f32x4 acc[4];
#pragma unroll
        for (int nt = 0; nt < 4; ++nt) acc[nt] = z;
#pragma unroll
        for (int k = 0; k < 3; ++k) {
          if (k < 2 || w < 4) {
            const v4u a = *(LAS v4u*)(yx + (i16 & 3) * 208 + k * 64 + g * 16);
#pragma unroll
            for (int nt = 0; nt < 4; ++nt) acc[nt] = mfma(a, wf[k][nt], acc[nt]);
          }
        }
        const float mw = addx16(sum4l(s1s)) * (w < 4 ? (1.f / 96.f) : (1.f / 64.f));
        float m2 = 0.f;
#pragma unroll
        for (int k = 0; k < 3; ++k) if (k < 2 || w < 4) { const f32x4 dd = xc[k] - mw; m2 += (dd.x * dd.x + dd.y * dd.y) + (dd.z * dd.z + dd.w * dd.w); }
        m2 = addx16(sum4l(m2));
        if ((lane & 0x3B) == 0) *(LAS f32x2*)(wst + (bb * 8 + w) * 2) = f32x2{mw, m2};
        const f32x4 a = g == 0 ? acc[0] : g == 1 ? acc[1] : g == 2 ? acc[2] : acc[3];
        *(LAS f32x2*)(red + ((w * 4 + g) * 16 + i16) * 2) = f32x2{a.x + a.y, a.z + a.w};
      }
      BAR();
      STAMP(12);
      if (w < 5) {
        float Ag = 0.f, Au = 0.f;
#pragma unroll
        for (int ww = 0; ww < 8; ++ww) { Ag += red[((ww * 4 + (fl >> 4)) * 16 + (fl & 15)) * 2 + fb]; Au += red[((ww * 4 + 2 + (fl >> 4)) * 16 + (fl & 15)) * 2 + fb]; }
        f32x4 st[4];
#pragma unroll
        for (int j = 0; j < 4; ++j) st[j] = *(LAS f32x4*)(wst + fb * 16 + j * 4);
        const float mu = ((st[0].x + st[0].z + st[1].x + st[1].z) * 96.f + (st[2].x + st[2].z + st[3].x + st[3].z) * 64.f) * (1.f / 640.f);
        float M2 = 0.f;
#pragma unroll
        for (int j = 0; j < 4; ++j) { const float n = j < 2 ? 96.f : 64.f; const float da = st[j].x - mu, db = st[j].z - mu; M2 += st[j].y + st[j].w + n * (da * da + db * db); }
        const float rsd = 1.f / sqrtf(M2 * (1.f / 640.f) + 1e-5f);
        const float gg = rsd * (Ag - mu * Bg) + Cg, uu = rsd * (Au - mu * Bu) + Cu;
        const unsigned hb = pk2(uu * sigm(gg), 0.f) & 0xFFFFu;
        f32x4 d = {0.f, 0.f, 0.f, 0.f};
#pragma unroll
        for (int t = 0; t < 8; ++t) {
          const unsigned hv = (unsigned)__builtin_amdgcn_ds_bpermute((((i16 >> 3) * 32 + 4 * t + g) * 4), (int)hb);
          d = mfma(diagfrag(hv, lane), wd[t], d);
        }
        st16(rs, WS_H6 + ((fg * 2 + (g >> 1)) * 640 + 128 * w + 8 * i16 + 4 * (g & 1)) * 4, tag4(d, tb));
      }
      STAMP(13);
      if (role9) {
        f32x4 x1v = {0.f, 0.f, 0.f, 0.f};
        if (w == 0) {
          const v2u u = ld8(rs, WS_H5 + (((lane >> 3) * 20 + (n9 >> 5)) * 2 + b9) * 64 + (lane & 7) * 8);
          f32x4 t = {blo(u.x), bhi(u.x), blo(u.y), bhi(u.y)};
#pragma unroll
          for (int j = 0; j < 4; ++j) { float a = t[j]; a += dppf<DPP_ROR8>(a); a = addx32(addx16(a)); t[j] = a; }
          x1v = f32x4{blo(x9.x), bhi(x9.x), blo(x9.y), bhi(x9.y)} + t;
        }
        nap<DLY_6>();
        const int pp = 7 * w + (lane >> 3);
        const bool act = (lane >> 3) < 7 && pp < 54;
        int off[1] = {WS_H6 + (((act ? pp : 0) * 2 + b9) * 640 + n9 + (lane & 7) * 4) * 4};
        v4u v[1];
        gather<1, 1>(rs, off, v, tb);
        STAMP(14);
        f32x4 t = untag4(v[0]);
#pragma unroll
        for (int j = 0; j < 4; ++j) { float a = act ? t[j] : 0.f; a += dppf<DPP_ROR8>(a); a = addx32(addx16(a)); t[j] = a; }
        if (lane < 8) *(LAS f32x4*)(red2 + (w * 8 + lane) * 4) = t;
        BAR();
        if (tid < 8) {
          f32x4 sm = {0.f, 0.f, 0.f, 0.f};
#pragma unroll
          for (int ww = 0; ww < 8; ++ww) sm += *(LAS f32x4*)(red2 + (ww * 8 + lane) * 4);
          const f32x4 o = x1v + gbv * sm;
          *(GAS v2u*)(P.out + b9 * 640 + n9 + lane * 4) = v2u{pk2(o.x, o.y), pk2(o.z, o.w)};
          if (fg == 0 && tid == 0) st8(rs, WS_EPOCH, v2u{tlo, thi});
        }
        STAMP(15);
      }
    }
  }
}

extern "C" void kernel_launch(void* const* d_in, const int* in_sizes, int n_in,
                              void* d_out, int out_size, void* d_ws, size_t ws_size,
                              hipStream_t stream) {
  Params p;
  p.x = (const u16*)d_in[0]; p.ckv = (const u16*)d_in[1]; p.wq = (const u16*)d_in[2]; p.wuk = (const u16*)d_in[3];
  p.wuv = (const u16*)d_in[4]; p.wo = (const u16*)d_in[5]; p.wg = (const u16*)d_in[7]; p.wu = (const u16*)d_in[8];
  p.wd = (const u16*)d_in[9]; p.c = (const u16*)d_in[10]; p.wmod = (const u16*)d_in[11];
  p.out = (u16*)d_out; p.ws = (unsigned char*)d_ws;
  mk_kernel<<<256, 512, 0, stream>>>(p);
}
```

```cpp
#include <hip/hip_runtime.h>
#include <hip/hip_bf16.h>
#include <stdint.h>

typedef unsigned short u16;
typedef unsigned long long u64;
typedef unsigned v4u __attribute__((ext_vector_type(4)));
typedef unsigned v2u __attribute__((ext_vector_type(2)));
typedef int v4i __attribute__((ext_vector_type(4)));
typedef float f32x4 __attribute__((ext_vector_type(4)));
typedef float f32x2 __attribute__((ext_vector_type(2)));
typedef short s16x4 __attribute__((ext_vector_type(4)));
typedef unsigned short u16x2 __attribute__((ext_vector_type(2)));
typedef __bf16 bf16x8_t __attribute__((ext_vector_type(8)));
typedef __bf16 bf16x2_t __attribute__((ext_vector_type(2)));
#define GAS __attribute__((address_space(1)))
#define LAS __attribute__((address_space(3)))
#define DI __device__ __forceinline__

constexpr int WS_EPOCH = 0;
constexpr int WS_TOK   = 4096;
constexpr int WS_HINT2 = 16384;
constexpr int WS_HINT3 = 32768;
constexpr int WS_HINT4 = 49152;
constexpr int WS_HINT5 = 65536;
constexpr int WS_H1    = 81920;
constexpr int WS_MR    = WS_H1 + 20480;
constexpr int WS_H3S   = WS_MR + 81920;
constexpr int WS_R1    = WS_H3S + 32768;
constexpr int WS_H2    = WS_R1;
constexpr int WS_H5    = WS_H2 + 131072;
constexpr int WS_H4    = WS_H5 + 20480;
constexpr int WS_H6    = WS_R1 + 245760;
static_assert(WS_H4 + 65536 <= WS_H6, "ws map");
constexpr int WS_H3    = 1 << 20;
constexpr int WS_BYTES = WS_H3 + (2 << 20);
constexpr int WS_STAMP = 4 << 20;
constexpr int NSTAMP = 28;
static_assert(WS_H6 + 276480 <= WS_H3, "ws map");

constexpr int L_TILE = 0;
constexpr int L_TILEB = 81920;
constexpr int LT_MISC = 114688;
constexpr int LA_Q = 131072;
constexpr int LA_P = LA_Q + 16 * 1040;
constexpr int LA_MISC = LA_P + 16 * 272;
constexpr int L_STAMP = 163840 - 256;
constexpr int LDS_BYTES = 163840;
static_assert(LA_MISC + 9216 + 1024 <= L_STAMP, "lds map");

#define DLY_P0 0
#define DLY_A0 0
#define DLY_2 0
#define DLY_3 40
#define DLY_4 0
#define DLY_5 0
#define DLY_6 12
#define NHELP 6
#define NWOW 10
#define RETRY_SLP 0
#define DLY_G 0
#define DLY_B 0
#define FRONT 0
#define CODE_WARM_R 26
#define ORDER 0
#define DLY_T 0
#define NBAND 0
#define DEP_P1 63
#define DEP_A1 0
#define DEP_2 63
template <int D> __device__ __forceinline__ void pace() { if (D < 63) asm volatile("s_waitcnt vmcnt(%0)" :: "i"(D) : "memory"); }
constexpr int SPIN_CAP = 1 << 18;
constexpr unsigned SENT = 0xFFFFFFFFu;

struct Params {
  const u16 *x, *ckv, *wq, *wuk, *wuv, *wo, *wg, *wu, *wd, *c, *wmod;
  u16* out;
  unsigned char* ws;
};

DI float asf(unsigned u) { return __builtin_bit_cast(float, u); }
DI unsigned asu(float f) { return __builtin_bit_cast(unsigned, f); }
DI unsigned pk2(float a, float b) { f32x2 v = {a, b}; return __builtin_bit_cast(unsigned, __builtin_convertvector(v, bf16x2_t)); }
DI float blo(unsigned w) { return asf(w << 16); }
DI float bhi(unsigned w) { return asf(w & 0xFFFF0000u); }
DI f32x4 mfma(v4u a, v4u b, f32x4 c) { return __builtin_amdgcn_mfma_f32_16x16x32_bf16(__builtin_bit_cast(bf16x8_t, a), __builtin_bit_cast(bf16x8_t, b), c, 0, 0, 0); }
DI float opq(float x) { asm("" : "+v"(x)); return x; }
#define PIN(x) asm volatile("" : "+v"(x))
template <int CTRL> DI unsigned dppu(unsigned v) { return (unsigned)__builtin_amdgcn_update_dpp(0, (int)v, CTRL, 0xf, 0xf, true); }
template <int CTRL> DI float dppf(float v) { return asf(dppu<CTRL>(asu(v))); }
template <int CTRL> DI float dadd(float v) { return opq(v + dppf<CTRL>(v)); }
constexpr int DPP_X1 = 0xB1, DPP_X2 = 0x4E, DPP_HMIR = 0x141, DPP_MIR = 0x140, DPP_ROR8 = 0x128, DPP_BC = 0x150;
DI float sum16l(float v) { return dadd<DPP_MIR>(dadd<DPP_HMIR>(dadd<DPP_X2>(dadd<DPP_X1>(v)))); }
DI float merge16(float a, float b) { auto s = __builtin_amdgcn_permlane16_swap(asu(a), asu(b), false, false); return opq(asf(s[0]) + asf(s[1])); }
DI float merge32(float a, float b) { auto s = __builtin_amdgcn_permlane32_swap(asu(a), asu(b), false, false); return opq(asf(s[0]) + asf(s[1])); }
DI float addx16(float v) { return merge16(v, v); }
DI float addx32(float v) { return merge32(v, v); }
DI float maxx16(float v) { auto s = __builtin_amdgcn_permlane16_swap(asu(v), asu(v), false, false); return fmaxf(asf(s[0]), asf(s[1])); }
DI float maxx32(float v) { auto s = __builtin_amdgcn_permlane32_swap(asu(v), asu(v), false, false); return fmaxf(asf(s[0]), asf(s[1])); }
DI float hsum4(f32x4 a) { const f32x2 t = f32x2{a.x, a.y} + f32x2{a.z, a.w}; return opq(t.x + t.y); }
DI float ex2(float v) { return __builtin_amdgcn_exp2f(v); }
DI float rcp(float v) { return __builtin_amdgcn_rcpf(v); }
DI float sigm(float v) { return rcp(1.f + ex2(-1.4426950408889634f * v)); }
#define WAIT_VM0() asm volatile("s_waitcnt vmcnt(0)" ::: "memory")
#define WAIT_LGKM0() asm volatile("s_waitcnt lgkmcnt(0)" ::: "memory")
#define BAR() asm volatile("s_waitcnt lgkmcnt(0)\n\ts_barrier" ::: "memory")
#define CFENCE() asm volatile("" ::: "memory")
template <int N> DI void nap() {
#pragma unroll
  for (int i = 0; i < N / 64; ++i) __builtin_amdgcn_s_sleep(64);
  if (N % 64) __builtin_amdgcn_s_sleep(N % 64);
}
DI void dma16(const void* base, unsigned voff, unsigned ldsaddr) {
  unsigned keep;
  asm volatile("s_mov_b32 %0, m0\n\ts_mov_b32 m0, %3\n\ts_nop 0\n\tglobal_load_lds_dwordx4 %1, %2 nt\n\ts_mov_b32 m0, %0"
               : "=&s"(keep) : "v"(voff), "s"(base), "s"(ldsaddr) : "memory");
}
#define WARM_POL "nt"
DI void warm16(const void* base, unsigned voff, unsigned ldsaddr) {
  unsigned keep;
  asm volatile("s_mov_b32 %0, m0\n\ts_mov_b32 m0, %3\n\ts_nop 0\n\tglobal_load_lds_dwordx4 %1, %2 " WARM_POL "\n\ts_mov_b32 m0, %0"
               : "=&s"(keep) : "v"(voff), "s"(base), "s"(ldsaddr) : "memory");
}
DI void dma16v(const void* lane_ptr, unsigned ldsaddr) {
  unsigned keep;
  asm volatile("s_mov_b32 %0, m0\n\ts_mov_b32 m0, %2\n\ts_nop 0\n\tglobal_load_lds_dwordx4 %1, off nt\n\ts_mov_b32 m0, %0"
               : "=&s"(keep) : "v"(lane_ptr), "s"(ldsaddr) : "memory");
}
typedef __amdgpu_buffer_rsrc_t rsrc_t;
DI v4u ld16(rsrc_t rs, int off) { return __builtin_amdgcn_raw_buffer_load_b128(rs, off, 0, 16); }
DI v2u ld8(rsrc_t rs, int off) { return __builtin_amdgcn_raw_buffer_load_b64(rs, off, 0, 16); }
DI void st16(rsrc_t rs, int off, v4u v) { __builtin_amdgcn_raw_buffer_store_b128(v, rs, off, 0, 16); }
DI void st8(rsrc_t rs, int off, v2u v) { __builtin_amdgcn_raw_buffer_store_b64(v, rs, off, 0, 16); }
DI void st4(rsrc_t rs, int off, unsigned v) { __builtin_amdgcn_raw_buffer_store_b32(v, rs, off, 0, 16); }
DI v4u ldnt16(const void* p) { return __builtin_nontemporal_load((const GAS v4u*)p); }
DI unsigned umin_(unsigned a, unsigned b) { return a < b ? a : b; }
DI unsigned umax_(unsigned a, unsigned b) { return a > b ? a : b; }
DI int imax_(int a, int b) { return a > b ? a : b; }
DI unsigned nosent(unsigned w) { return umin_(w, 0xFFFFFFFEu); }
DI unsigned tagf(float f, unsigned tb) { return (asu(f) & 0xFFFFFF00u) | tb; }
DI v4u tag4(f32x4 f, unsigned tb) { return v4u{tagf(f.x, tb), tagf(f.y, tb), tagf(f.z, tb), tagf(f.w, tb)}; }
DI f32x4 untag4(v4u v) { return f32x4{asf(v.x & 0xFFFFFF00u), asf(v.y & 0xFFFFFF00u), asf(v.z & 0xFFFFFF00u), asf(v.w & 0xFFFFFF00u)}; }
DI unsigned bad0(v4u v) { return umax_(umax_(v.x, v.y), umax_(v.z, v.w)); }
DI unsigned bad1(v4u v, unsigned tb) { return __builtin_amdgcn_bitop3_b32(v.x, v.y, tb, 0x7E) | __builtin_amdgcn_bitop3_b32(v.z, v.w, tb, 0x7E); }
DI unsigned bad2(v4u v, unsigned t) { return __builtin_amdgcn_bitop3_b32(v.y, v.w, t, 0x7E); }

template <int N, int MODE0, int NB = 0, int SLP = RETRY_SLP>
DI void gather(rsrc_t rs, const int (&off)[N + NB], v4u (&v)[N + NB], unsigned t0, unsigned t1 = 0) {
#pragma unroll 1
  for (int spins = 0; spins < SPIN_CAP; ++spins) {
    CFENCE();
#pragma unroll
    for (int i = 0; i < N + NB; ++i) v[i] = ld16(rs, off[i]);
    unsigned a = 0, b = 0;
#pragma unroll
    for (int i = 0; i < N; ++i) a = MODE0 == 0 ? umax_(a, bad0(v[i])) : (a | bad1(v[i], t0));
#pragma unroll
    for (int i = N; i < N + NB; ++i) b |= bad2(v[i], t1);
    const bool ok = (MODE0 == 0 ? a != SENT : (a & 0xFFu) == 0u) && b == 0u;
    if (__all(ok)) break;
    if (SLP) __builtin_amdgcn_s_sleep(SLP);
  }
  CFENCE();
}
DI void wait_tag64(rsrc_t rs, int off, unsigned tlo, unsigned thi) {
#pragma unroll 1
  for (int spins = 0; spins < SPIN_CAP; ++spins) {
    CFENCE();
    const v2u v = ld8(rs, off);
    if (__all(v.x == tlo && v.y == thi)) break;
    __builtin_amdgcn_s_sleep(1);
  }
  CFENCE();
}
DI v4u trfrag(LAS unsigned char* L, unsigned a0, unsigned a1) {
  const s16x4 x = __builtin_amdgcn_ds_read_tr16_b64_v4i16((LAS s16x4*)(L + a0));
  const s16x4 y = __builtin_amdgcn_ds_read_tr16_b64_v4i16((LAS s16x4*)(L + a1));
  const v2u xx = __builtin_bit_cast(v2u, x), yy = __builtin_bit_cast(v2u, y);
  return v4u{xx.x, xx.y, yy.x, yy.y};
}
DI v4u diag_sel(int lane, unsigned hsel) {
  const unsigned two = ((2 * hsel + 1) << 8) | (2 * hsel);
  const unsigned s = (lane & 1) ? ((two << 16) | 0x0C0Cu) : (0x0C0C0000u | two);
  const int d = (lane & 7) >> 1;
  return v4u{d == 0 ? s : 0x0C0C0C0Cu, d == 1 ? s : 0x0C0C0C0Cu, d == 2 ? s : 0x0C0C0C0Cu, d == 3 ? s : 0x0C0C0C0Cu};
}
DI v4u diag_mk(unsigned v, v4u sel) {
  return v4u{__builtin_amdgcn_perm(0u, v, sel.x), __builtin_amdgcn_perm(0u, v, sel.y), __builtin_amdgcn_perm(0u, v, sel.z), __builtin_amdgcn_perm(0u, v, sel.w)};
}
DI v4u and4(v4u a, unsigned m) { return v4u{a.x & m, a.y & m, a.z & m, a.w & m}; }

#define STAMP(k) do { } while (0)
#define STAMPX(k) do { } while (0)

#define OST(k) STAMP(k)
#define GST(k) STAMP(k)
#define GSX(k) STAMPX(k)
#define S1ST(k)
#define PST(k)
template <int NI, bool TWO = false, int DEP = 63>
DI void tile_dma(const void* baseL, const void* baseR, unsigned stride, int k0, int lane, unsigned ldsbase, int rot) {
  const unsigned r8 = lane >> 3, cpos = lane & 7;
  const unsigned chE = (((cpos >> 1) ^ ((r8 >> 1) & 1u)) << 1) | (cpos & 1), chO = chE ^ 4u;
  if (TWO) {
    const char* pE = (const char*)((chE >> 2) ? baseR : baseL) + (r8 * stride + (chE & 3) * 16);
    const char* pO = (const char*)((chO >> 2) ? baseR : baseL) + (r8 * stride + (chO & 3) * 16);
#pragma unroll
    for (int i = 0; i < NI; ++i) {
      int ii = i + rot; if (ii >= NI) ii -= NI;
      dma16v(((i & 1) ? pO : pE) + (size_t)((unsigned)(k0 + 8 * ii) * stride), ldsbase + (unsigned)(k0 + 8 * ii) * 128u);
      pace<DEP>();
    }
  } else {
    const unsigned vE = r8 * stride + chE * 16, vO = r8 * stride + chO * 16;
#pragma unroll
    for (int i = 0; i < NI; ++i) {
      int ii = i + rot; if (ii >= NI) ii -= NI;
      dma16((const char*)baseL + (size_t)((unsigned)(k0 + 8 * ii) * stride), (i & 1) ? vO : vE, ldsbase + (unsigned)(k0 + 8 * ii) * 128u);
      pace<DEP>();
    }
  }
}
template <int NS>
DI void tile_frags(LAS unsigned char* L, unsigned ldsoff, int k0, int lane, v4u (&w)[3][4]) {
  const unsigned g = lane >> 4, q = (lane & 15) >> 2, p = lane & 3;
  const unsigned f = (q >> 1) | ((g & 1) << 1);
  const unsigned base = ldsoff + (k0 + 8 * g + q) * 128 + 8 * p;
#pragma unroll
  for (int s = 0; s < NS; ++s)
#pragma unroll
    for (int nt = 0; nt < 4; ++nt) {
      const unsigned a = base + s * 4096 + ((nt ^ f) * 32);
      w[s][nt] = trfrag(L, a, a + 512);
    }
}
struct Row { v4u a, b; };
DI Row row_load(const u16* row, int lane) {
  Row r; r.a = *(const GAS v4u*)(row + lane * 8); r.b = *(const GAS v4u*)(row + 512 + (lane & 15) * 8); return r;
}
DI float wave_sum(float v) { return addx32(addx16(sum16l(v))); }
DI float sum8bf(v4u v) { return ((blo(v.x) + bhi(v.x)) + (blo(v.y) + bhi(v.y))) + ((blo(v.z) + bhi(v.z)) + (blo(v.w) + bhi(v.w))); }
DI float dev8bf(v4u v, float mu) {
  float s = 0.f;
#pragma unroll
  for (int j = 0; j < 4; ++j) { const unsigned e = v[j]; const float a = blo(e) - mu, b = bhi(e) - mu; s += a * a + b * b; }
  return s;
}
DI float row_mean(const Row& r, int lane) { return wave_sum(sum8bf(r.a) + (lane < 16 ? sum8bf(r.b) : 0.f)) * (1.f / 640.f); }
DI float row_var(const Row& r, int lane, float mu) { return wave_sum(dev8bf(r.a, mu) + (lane < 16 ? dev8bf(r.b, mu) : 0.f)) * (1.f / 640.f); }
DI float guard_oc(float oc) { return fabsf(oc) < 1e-18f ? 1e-18f : oc; }

DI unsigned silu_pair(unsigned c0, unsigned c1) { const float a = asf(c0 << 16), b = asf(c1 << 16); return pk2(a * sigm(a), b * sigm(b)); }
DI f32x4 mod_mfma(unsigned sp, v4u sel, const v4u (&wm)[5]) {
  f32x4 d = mfma(diag_mk(dppu<DPP_BC + 0>(sp), sel), wm[0], f32x4{0.f, 0.f, 0.f, 0.f});
  d = mfma(diag_mk(dppu<DPP_BC + 1>(sp), sel), wm[1], d);
  d = mfma(diag_mk(dppu<DPP_BC + 2>(sp), sel), wm[2], d);
  d = mfma(diag_mk(dppu<DPP_BC + 3>(sp), sel), wm[3], d);
  return mfma(diag_mk(dppu<DPP_BC + 4>(sp), sel), wm[4], d);
}
#define CW_STR 64
constexpr int CODE_WARM_LINES = 25600 / CW_STR;

__global__ void __launch_bounds__(512, 2) mk_kernel(Params P) {
  __shared__ __attribute__((aligned(16))) unsigned char smem[LDS_BYTES];
  LAS unsigned char* L = (LAS unsigned char*)smem;
  const unsigned Lb = (unsigned)(size_t)L;
  const int tid = threadIdx.x, lane = tid & 63;
  const int w = __builtin_amdgcn_readfirstlane(tid >> 6);
  const int bid = blockIdx.x, xc = bid & 7, r = bid >> 3;
  const int g = lane >> 4, i16 = lane & 15;
  if (w < (CODE_WARM_LINES + 63) / 64 && r >= CODE_WARM_R) {
    const u64 pc = __builtin_amdgcn_s_getpc() & ~127ull;
    unsigned keep;
    asm volatile("s_mov_b32 %0, m0\n\ts_mov_b32 m0, %3\n\ts_nop 0\n\tglobal_load_lds_dword %1, %2\n\ts_mov_b32 m0, %0"
                 : "=&s"(keep) : "v"((tid < CODE_WARM_LINES ? tid : 0) * CW_STR), "s"(pc), "s"(Lb + L_STAMP - 1024) : "memory");
  }
  const bool isS1 = (unsigned)(r - 16) < 10u && !(r & 1);
  const int k0w = w < 4 ? 96 * w : 128 + 64 * w;
  const int slot = ((r - 16) >> 1) * 8 + xc, cgp = ((r - 16) & 14) + (xc & 1), jq = xc >> 1;
  const int mfb = w >> 2, mfm = 16 * (w & 3) + (lane >> 2), mty = (lane >> 1) & 1;
  unsigned xs0, xs1, xs2, xs3, xs4, xm, c0s, c1s;
  const rsrc_t rs = __builtin_amdgcn_make_buffer_rsrc((void*)P.ws, 0, WS_BYTES, 0x00020000);
  v2u ep;
  {
    const bool isP = r < 16;
    const int hi = r < 26 ? (r - 16) >> 1 : r - 21;
    unsigned lS = w * 5120, lP = k0w * 128;
    unsigned oS = (8 * jq + w) * 153600 + 128 * cgp, oP = lP * 64 + xc * 1024 + (r >> 1) * 128, oA = (hi * 8 + w) * 8192 + xc * 1024;
    asm("" : "+s"(lS), "+s"(lP), "+s"(oS), "+s"(oP), "+s"(oA));
    const unsigned off = isS1 ? oS : isP ? oP : oA;
    unsigned lds = Lb + (isS1 ? lS : isP ? lP : (unsigned)(L_STAMP - 1024));
    const unsigned lstep = (isS1 || isP) ? 1024u : 0u;
    const unsigned step = isS1 ? 30720u : isP ? 65536u : 88u * 8192u;
    const int cnt = isS1 ? 5 : isP ? (w < 4 ? 12 : 8) : ((hi < 9 || NHELP < 6) ? NHELP : NHELP - 1);
    const unsigned xmask = isP ? 0x40u : 0u;
    unsigned vS = g * 7680u + (i16 >> 3) * 1280u + (i16 & 7) * 16u;
    unsigned vP = (lane >> 3) * 8192u + (((lane & 7) ^ (g & 1) * 2) * 16u);
    unsigned vA = lane * 16u;
    unsigned vx = mfb * 1280 + lane * 4, vm = (mfb * 640 + 64 * cgp + mfm) * 2, vc = (160 * jq + 20 * w + g + 4 * (i16 < 4 ? i16 : 4)) * 2;
    unsigned vz = (isS1 && w >= 2) ? WS_R1 + slot * 6144 + (tid - 128) * 16 : WS_BYTES;
    asm("" : "+v"(vS), "+v"(vP), "+v"(vA));
    const unsigned vb = isS1 ? vS : isP ? vP : vA;
    if (DLY_P0 + DLY_B > 0 && !isS1) { if (isP) nap<DLY_P0>(); else nap<DLY_B>(); }
    asm volatile("" :: "s"(P.x), "s"(P.ckv), "s"(P.wq), "s"(P.wuk), "s"(P.c), "s"(P.wmod), "s"(P.ws), "s"(off), "s"(lds), "s"(step), "s"(cnt), "s"(xmask), "s"(lstep), "v"(vb), "v"(vx), "v"(vm), "v"(vc), "v"(vz));
    if (isS1) {
      asm volatile("global_load_ushort %6, %10, %12\n\tglobal_load_ushort %7, %10, %12 offset:1280\n\t"
                   "global_load_dword %0, %8, %11\n\tglobal_load_dword %1, %8, %11 offset:256\n\tglobal_load_dword %2, %8, %11 offset:512\n\t"
                   "global_load_dword %3, %8, %11 offset:768\n\tglobal_load_dword %4, %8, %11 offset:1024\n\tglobal_load_ushort %5, %9, %11"
                   : "=&v"(xs0), "=&v"(xs1), "=&v"(xs2), "=&v"(xs3), "=&v"(xs4), "=&v"(xm), "=&v"(c0s), "=&v"(c1s)
                   : "v"(vx), "v"(vm), "v"(vc), "s"(P.x), "s"(P.c) : "memory");
    }
    if (!isS1 && !isP && hi < 10) {
      const unsigned sc = (3u - ((unsigned)(size_t)P.wq >> 7)) & 7u;
      dma16((const char*)P.wq + ((hi * 8 + w) * 65536 + xc * 1024 + sc * 128), vP, lds);
    }
    const char* base = (const char*)(isS1 ? P.wmod : isP ? P.wq : P.wuk) + off;
#pragma unroll 1
    for (int i = 0; i < cnt; ++i) {
      dma16(base, vb ^ ((i << 6) & xmask), lds);
      base += step; lds += lstep;
    }
    ep = ld8(rs, WS_EPOCH);
    if (isS1) st16(rs, vz, v4u{SENT, SENT, SENT, SENT});
    CFENCE();
  }
  S1ST(21);
  if (isS1) {
    v4u sel = diag_sel(lane, i16 >> 3);
    unsigned rw = LA_MISC + ((w >> 2) * 256 + lane) * 16 + (w & 3) * 4;
    unsigned rr = LA_MISC + ((lane & 1) * 256 + (mfm & 3) * 64 + (2 * mfb + ((mfm >> 2) & 1)) * 16 + (mfm >> 3) + 8 * mty) * 16;
    int o1 = WS_H1 + ((((2 * cgp + (mfm >> 5)) * 2 + mfb) * 4 + jq) * 32 + (mfm & 31)) * 4;
    PIN(sel); PIN(rw); PIN(rr); PIN(o1);
    S1ST(17);
    asm volatile("s_waitcnt vmcnt(7)" : "+v"(xs0), "+v"(xs1), "+v"(xs2), "+v"(xs3), "+v"(xs4), "+v"(xm), "+v"(c0s), "+v"(c1s) :: "memory");
    unsigned sp = silu_pair(c0s, c1s);
    const f32x2 e0 = {blo(xs0), bhi(xs0)}, e1 = {blo(xs1), bhi(xs1)}, e2 = {blo(xs2), bhi(xs2)}, e3 = {blo(xs3), bhi(xs3)}, e4 = {blo(xs4), bhi(xs4)};
    const f32x2 es = ((e0 + e1) + (e2 + e3)) + e4;
    const float mu = wave_sum(es.x + es.y) * (1.f / 640.f);
    const f32x2 d0 = e0 - mu, d1 = e1 - mu, d2 = e2 - mu, d3 = e3 - mu, d4 = e4 - mu;
    const f32x2 q2 = d0 * d0 + d1 * d1 + d2 * d2 + d3 * d3 + d4 * d4;
    const float rsd = __builtin_amdgcn_rsqf(wave_sum(q2.x + q2.y) * (1.f / 640.f) + 1e-5f);
    float xn = (asf(xm << 16) - mu) * rsd;
    PIN(sp); PIN(xn);
    S1ST(18);
    WAIT_VM0();
    v4u wm[5];
#pragma unroll
    for (int t = 0; t < 5; ++t) wm[t] = *(LAS v4u*)(L + (w * 5 + t) * 1024 + lane * 16);
    const f32x4 d = mod_mfma(sp, sel, wm);
    v2u e2_ = ep; PIN(e2_);
    const unsigned elo = __builtin_amdgcn_readfirstlane(e2_.x), ehi = __builtin_amdgcn_readfirstlane(e2_.y);
    const unsigned tl = elo + 1u, th = ehi + (tl == 0u ? 1u : 0u);
#pragma unroll
    for (int j = 0; j < 4; ++j) *(LAS float*)(L + rw + j * 1024) = d[j];
    S1ST(19);
    BAR();
    S1ST(20);
    const float v = dadd<DPP_X1>(hsum4(*(LAS f32x4*)(L + rr)));
    const float hp = xn * (dppf<DPP_X2>(v) + (jq == 0 ? 1.f : 0.f)) + v;
    if ((lane & 3) == 0) st4(rs, o1, pk2(0.f, hp) | (tl & 0xFFu));
    if (tid == 1) st8(rs, WS_TOK + slot * 128, v2u{tl, th});
    STAMP(1);
  }
  const v4u SENT4 = v4u{SENT, SENT, SENT, SENT};
  const f32x4 Z4 = {0.f, 0.f, 0.f, 0.f};
  const int par = r & 1;
  const unsigned Ldump = Lb + L_STAMP - 1024;
  unsigned tlo, thi, tb;

  if (r < 16) {
    const int h = 4 * xc + (r >> 2), sub = r & 3, dh = sub >> 1, ch = sub & 1, hg = xc >> 2;
    const int pidx = xc * 16 + r;
    {
      const u16* wk = P.wuk + h * 128 + dh * 64 + (size_t)(256 * ch + 32 * w) * 4096;
#pragma unroll
      for (int i = 0; i < 4; ++i) {
        const unsigned lr = 8 * i + (lane >> 3);
        dma16(wk, lr * 8192u + (((lane & 7) ^ ((lr >> 1) & 7u)) * 16), Lb + L_TILEB + w * 4096 + i * 1024);
      }
      const int c3 = WS_H3 + (h * 4 + sub) * 16384 + tid * 16;
      st16(rs, c3, SENT4); st16(rs, c3 + 8192, SENT4);
    }
    const int nl = xc < 3 ? 4 : 3, lc0 = 3 * xc + (xc < 3 ? xc : 3);
    const int fg = 2 * (lc0 + (r >> 2)) + par;
    WAIT_VM0();
    STAMP(1); STAMPX(9);
    {
      const unsigned elo = __builtin_amdgcn_readfirstlane(ep.x), ehi = __builtin_amdgcn_readfirstlane(ep.y);
      tlo = elo + 1u; thi = ehi + (tlo == 0u ? 1u : 0u); tb = tlo & 0xFFu;
    }
    {
      v4u wf[3][4];
      v4u uk[2][2];
      if (w < 4) tile_frags<3>(L, L_TILE, k0w, lane, wf);
      else       tile_frags<2>(L, L_TILE, k0w, lane, wf);
#pragma unroll
      for (int u = 0; u < 2; ++u)
#pragma unroll
        for (int t = 0; t < 2; ++t) uk[u][t] = *(LAS v4u*)(L + L_TILEB + w * 4096 + (2 * i16 + u) * 128 + (((4 * t + g) ^ (i16 & 7)) * 16));
      const int o = WS_H1 + (k0w >> 5) * 1024 + lane * 16;
      int off[3] = {o, o + 1024, w < 4 ? o + 2048 : o};
      unsigned xbw = LT_MISC + 4096 + w * 1664 + (lane >> 3) * 208 + (lane & 7) * 8;
      unsigned xbr = LT_MISC + 4096 + w * 1664 + (i16 & 7) * 208 + g * 16;
      unsigned qxw = LT_MISC + (8 * (g & 1) + w) * 144 + ((g >> 1) * 32 + i16) * 2;
      unsigned qxr = LT_MISC + i16 * 144 + g * 16;
      int o2 = WS_H2 + ((((g >> 1) * 32 + h) * 2 + dh) * 512 + 256 * ch + 32 * w + 2 * i16) * 2;
      PIN(off[0]); PIN(off[2]); PIN(xbw); PIN(xbr); PIN(qxw); PIN(qxr); PIN(o2);
      WAIT_LGKM0();
      STAMP(2);
      v4u v[3];
      gather<3, 1>(rs, off, v, tb);
      if (tid == 0) st8(rs, WS_HINT2 + pidx * 128, v2u{tlo, thi});
      STAMP(3); STAMPX(10);
#pragma unroll
      for (int s = 0; s < 3; ++s) *(LAS v2u*)(L + xbw + 64 * s) = v2u{__builtin_amdgcn_perm(v[s].y, v[s].x, 0x07060302u), __builtin_amdgcn_perm(v[s].w, v[s].z, 0x07060302u)};
      WAIT_LGKM0();
      f32x4 acc[4];
#pragma unroll
      for (int s = 0; s < 3; ++s) {
        if (s < 2 || w < 4) {
          const v4u a = *(LAS v4u*)(L + xbr + 64 * s);
#pragma unroll
          for (int nt = 0; nt < 4; ++nt) acc[nt] = mfma(a, wf[s][nt], s == 0 ? Z4 : acc[nt]);
        }
      }
      const float sc = 0.08838834764831845f * 1.4426950408889634f;
      const float h0 = hsum4(acc[0]), h1 = hsum4(acc[1]), h2 = hsum4(acc[2]), h3 = hsum4(acc[3]);
      const unsigned pe = pk2((g < 2 ? h0 : h2) * sc, (g < 2 ? h1 : h3) * sc);
      *(LAS u16*)(L + qxw) = (u16)(pe & 0xFFFFu); *(LAS u16*)(L + qxw + 32) = (u16)(pe >> 16);
      BAR();
      STAMP(7);
      const v4u qa0 = *(LAS v4u*)(L + qxr), qa1 = *(LAS v4u*)(L + qxr + 64);
      f32x4 d0 = mfma(qa0, uk[0][0], Z4), d1 = mfma(qa0, uk[1][0], Z4);
      d0 = mfma(qa1, uk[0][1], d0); d1 = mfma(qa1, uk[1][1], d1);
      const unsigned ov = nosent(pk2(addx16(hsum4(d0)), addx16(hsum4(d1))));
      if ((g & 1) == 0) st4(rs, o2, ov);
    }
    STAMP(4);
    if (sub < 2) {
      {
        const u16* wv = P.wuv + (size_t)(256 * sub) * 4096 + h * 128;
        tile_dma<4>(wv, wv + 32, 8192, 32 * w, lane, Lb + L_TILE, 0);
        tile_dma<4>(wv + 64, wv + 96, 8192, 32 * w, lane, Lb + L_TILE + 32768, 0);
      }
      WAIT_VM0();
      BAR();
      STAMP(5);
      v4u uv[8];
      {
        const unsigned q = i16 >> 2, p = lane & 3;
#pragma unroll
        for (int t = 0; t < 8; ++t) {
          const unsigned f = (g >> 1) | (((t >> 1) & 1) << 1);
          const unsigned a = L_TILE + (w >> 2) * 32768 + (128 * (t >> 2) + 16 * q + 4 * (t & 3) + g) * 128 + 8 * p + (((w & 3) ^ f) * 32);
          uv[t] = trfrag(L, a, a + 8192);
        }
      }
      const int b = w >> 2, sq = w & 3;
      const int od = WS_H3 + (h * 4 + 2 * sub) * 16384 + (b * 32 + 8 * sq) * 256 + lane * 16;
      const int os = WS_H3S + (((b * 2 + hg) * 32 + 8 * sq + g) * 16 + (h & 15)) * 16;
      int off[6] = {od, od + 1024, od + 16384, od + 17408, os, os + 1024};
      v4u sl0 = diag_sel(lane, 0), sl1 = diag_sel(lane, 1);
      unsigned x5w = LT_MISC + 4096 + w * 528 + (g >> 1) * 256 + (8 * i16 + 4 * (g & 1)) * 2;
      unsigned x5r = LT_MISC + 4096 + (i16 & 7) * 528 + g * 16;
      unsigned str = LT_MISC + 8704 + (g & 1) * 32;
      int o4 = WS_H4 + ((((h * 2 + (w >> 2)) * 2 + (g & 1)) * 2 + sub) * 64 + 16 * (w & 3) + i16) * 2;
      PIN(off[0]); PIN(off[2]); PIN(off[4]); PIN(sl0); PIN(sl1); PIN(x5w); PIN(x5r); PIN(str); PIN(o4);
      WAIT_LGKM0();
      STAMP(25);
      wait_tag64(rs, WS_HINT3 + ((((pidx * 2 + (lane & 1) * 33) & 63) * 2) + hg) * 128, tlo, thi);
      STAMP(26);
      nap<DLY_3>();
      v4u v[6];
      gather<4, 0, 2>(rs, off, v, 0u, tlo);
      if (tid == 0) st8(rs, WS_HINT4 + (h * 2 + sub) * 128, v2u{tlo, thi});
      STAMP(11);
      const float m0 = asf(v[4].x), l0 = asf(v[4].z), m1 = asf(v[5].x), l1 = asf(v[5].z);
      const float mw = maxx32(maxx16(fmaxf(m0, m1)));
      const unsigned pe = pk2(ex2(m0 - mw), ex2(m1 - mw));
      const v4u a0 = diag_mk(pe, sl0), a1 = diag_mk(pe, sl1);
      f32x4 d0 = mfma(a0, v[0], Z4), d1 = mfma(a0, v[2], Z4);
      d0 = mfma(a1, v[1], d0); d1 = mfma(a1, v[3], d1);
      const float lw = addx32(addx16(blo(pe) * l0 + bhi(pe) * l1));
      const f32x4 d = g < 2 ? d0 : d1;
      *(LAS v2u*)(L + x5w) = v2u{pk2(d.x, d.y), pk2(d.z, d.w)};
      if (lane == 0) *(LAS f32x2*)(L + LT_MISC + 8704 + w * 8) = f32x2{mw, lw};
      BAR();
      STAMP(12);
      const f32x4 sa = *(LAS f32x4*)(L + str), sb = *(LAS f32x4*)(L + str + 16);
      f32x4 e0 = Z4, e1 = Z4;
#pragma unroll
      for (int t = 0; t < 4; ++t) { e0 = mfma(*(LAS v4u*)(L + x5r + t * 64), uv[t], e0); e1 = mfma(*(LAS v4u*)(L + x5r + 256 + t * 64), uv[4 + t], e1); }
      const float M = fmaxf(fmaxf(sa.x, sa.z), fmaxf(sb.x, sb.z));
      const float f0 = ex2(sa.x - M), f1 = ex2(sa.z - M), f2 = ex2(sb.x - M), f3 = ex2(sb.z - M);
      const float rl = rcp(f0 * sa.y + f1 * sa.w + f2 * sb.y + f3 * sb.w);
      const f32x4 e = e0 + e1;
      const float val = opq((f0 * e.x + f1 * e.y + f2 * e.z + f3 * e.w) * rl);
      const unsigned ov = nosent(pk2(val, dppf<DPP_X1>(val)));
      if (g < 2 && (i16 & 1) == 0) st4(rs, o4, ov);
      STAMP(24);
    } else {
    const bool role7 = (r >> 2) < nl;
    const u16* wg = P.wg + 32 * (role7 ? fg : 0); const u16* wu = P.wu + 32 * (role7 ? fg : 0);
    {
      const unsigned cl = (lane & 31) < 8 * nl ? (lane & 31) : 0;
      const char* gsrc = (const char*)(((w & 1) ? P.wu : P.wg) + 64 * lc0 + (size_t)(80 * ((r >> 2) * 2 + par) + 20 * (w >> 1)) * 1728);
#pragma unroll 1
      for (int i = 0; i < NBAND; ++i) dma16(gsrc + i * 6912, (lane >> 5) * 3456u + cl * 16u, Ldump);
    }
    if (role7) {
    const bool role9 = fg < 40;
    const int b9 = role9 ? fg / 20 : 0, p9 = (fg % 20) * 32;
    const int nb9 = (p9 & ~127) + 2 * (p9 & 63) + 4 * ((p9 >> 6) & 1);
    LAS unsigned char* YS = L + L_TILEB;
    LAS float* ROC = (LAS float*)(L + L_TILEB + 16384);
    LAS float* XC = ROC + 1280;
    LAS float* GB = XC + 1280;
    constexpr int T_RED = LT_MISC, T_WST = LT_MISC + 12288, T_HH = T_WST + 512, T_R9 = T_HH + 1024, T_XB = T_R9 + 1024;
    static_assert(T_XB + 8 * 16 * 208 <= L_STAMP - 1024, "tail lds");
    LAS float* red = (LAS float*)(L + T_RED);
    LAS float* wst = (LAS float*)(L + T_WST);
    const int e9 = (tid >> 1) & 31, n9 = nb9 + 8 * (e9 >> 2) + (e9 & 3);
    const bool mainT = tid < 320;
    const int bb = mainT ? tid / 160 : b9, m4 = mainT ? (tid % 160) * 4 : 0;
    const Row xr0 = row_load(P.x, lane), xr1 = row_load(P.x + 640, lane);
    const v2u xr = *(const GAS v2u*)(P.x + bb * 640 + m4);
    float x9 = asf((unsigned)P.x[b9 * 640 + n9] << 16);
    CFENCE();
    if (w < 4) tile_dma<12, true>(wg, wu, 3456, k0w, lane, Lb + L_TILE, par * 6);
    else       tile_dma<8, true>(wg, wu, 3456, k0w, lane, Lb + L_TILE, par * 4);
    v4u wd[8];
    if (w < 5) {
#pragma unroll
      for (int j = 0; j < 8; ++j) wd[j] = ldnt16(P.wd + (size_t)(32 * fg + 8 * g + j) * 640 + 128 * w + 8 * i16);
    }
    v4u mv[8];
    if (w < 6) {
      const int cA = mainT ? 640 + m4 : 1920 + nb9 + (tid & 7) * 8, cB = mainT ? 1280 + m4 : cA;
      int off[8];
#pragma unroll
      for (int p = 0; p < 4; ++p) { off[p] = WS_MR + ((p * 2 + bb) * 2560 + cA) * 4; off[4 + p] = WS_MR + ((p * 2 + bb) * 2560 + cB) * 4; }
      gather<8, 1, 0, 12>(rs, off, mv, tb);
    }
    WAIT_VM0();
    GSX(21);
    v4u wf[3][4];
    if (w < 4) tile_frags<3>(L, L_TILE, k0w, lane, wf);
    else       tile_frags<2>(L, L_TILE, k0w, lane, wf);
    v4u wt[8];
    if (w < 6) {
      const float mean0 = row_mean(xr0, lane), mean1 = row_mean(xr1, lane);
      const float piv = bb ? mean1 : mean0;
      const f32x4 sb = (untag4(mv[0]) + untag4(mv[1])) + (untag4(mv[2]) + untag4(mv[3]));
      const f32x4 cb = (untag4(mv[4]) + untag4(mv[5])) + (untag4(mv[6]) + untag4(mv[7]));
      if (mainT) {
        const f32x4 oc = {guard_oc(1.f + cb.x), guard_oc(1.f + cb.y), guard_oc(1.f + cb.z), guard_oc(1.f + cb.w)};
        const f32x4 xcv = {blo(xr.x) - piv, bhi(xr.x) - piv, blo(xr.y) - piv, bhi(xr.y) - piv};
        *(LAS f32x4*)(ROC + bb * 640 + m4) = f32x4{rcp(oc.x), rcp(oc.y), rcp(oc.z), rcp(oc.w)};
        *(LAS f32x4*)(XC + bb * 640 + m4) = xcv;
#pragma unroll
        for (int ty = 0; ty < 3; ++ty) {
          const f32x4 y = ty == 0 ? oc : ty == 1 ? sb : xcv * oc;
          const unsigned h0 = pk2(y.x, y.y), h1 = pk2(y.z, y.w);
          *(LAS v2u*)(YS + (ty * 4 + bb * 2) * 1296 + m4 * 2) = v2u{h0, h1};
          *(LAS v2u*)(YS + (ty * 4 + bb * 2 + 1) * 1296 + m4 * 2) = v2u{pk2(y.x - blo(h0), y.y - bhi(h0)), pk2(y.z - blo(h1), y.w - bhi(h1))};
        }
      } else if (tid < 328) {
        *(LAS f32x4*)(GB + (tid & 7) * 4) = sb;
      }
      if (w < 5) {
#pragma unroll
        for (int t = 0; t < 8; ++t)
#pragma unroll
          for (int d = 0; d < 4; ++d) wt[t][d] = __builtin_amdgcn_perm(wd[2 * d + 1][t >> 1], wd[2 * d][t >> 1], (t & 1) ? 0x07060302u : 0x05040100u);
      }
#pragma unroll
      for (int t = 0; t < 8; ++t) PIN(wt[t]);
    }
    BAR();
    GST(13);
    {
      f32x4 acc[4];
#pragma unroll
      for (int s = 0; s < 3; ++s) {
        if (s < 2 || w < 4) {
          const v4u a = *(LAS v4u*)(YS + (i16 < 12 ? i16 : i16 - 4) * 1296 + (k0w + 32 * s + 8 * g) * 2);
#pragma unroll
          for (int nt = 0; nt < 4; ++nt) acc[nt] = mfma(a, wf[s][nt], s == 0 ? Z4 : acc[nt]);
        }
      }
      if (g < 3) {
#pragma unroll
        for (int nt = 0; nt < 4; ++nt) {
          red[((g * 2 + (w >> 2)) * 128 + 16 * nt + i16) * 4 + (w & 3)] = acc[nt].x + acc[nt].y;
          red[((g * 2 + (w >> 2)) * 128 + 64 + 16 * nt + i16) * 4 + (w & 3)] = acc[nt].z + acc[nt].w;
        }
      }
    }
    float roc[3], xcs[3];
#pragma unroll
    for (int s = 0; s < 3; ++s) {
      const int k = k0w + ((s < 2 || w < 4) ? 32 * s : 0) + 16 * (g & 1) + i16;
      roc[s] = ROC[(g >> 1) * 640 + k]; xcs[s] = XC[(g >> 1) * 640 + k];
    }
    float gb9 = GB[e9], roc9 = ROC[b9 * 640 + n9];
    BAR();
    const int fl = lane & 31, fb = lane >> 5;
    float cons[3][2];
#pragma unroll
    for (int ty = 0; ty < 3; ++ty)
#pragma unroll
      for (int gu = 0; gu < 2; ++gu)
        cons[ty][gu] = hsum4(*(LAS f32x4*)(red + ((ty * 2 + 0) * 128 + fb * 64 + gu * 32 + fl) * 4)) + hsum4(*(LAS f32x4*)(red + ((ty * 2 + 1) * 128 + fb * 64 + gu * 32 + fl) * 4));
    v4u idf[2];
#pragma unroll
    for (int u = 0; u < 2; ++u) {
      const bool on = (g >> 1) == u && (g & 1) == (i16 >> 3);
      idf[u] = diag_mk(on ? 0x3F80u : 0u, diag_sel(lane, 0));
    }
    const int o5 = WS_H5 + (k0w >> 5) * 1024 + lane * 16;
    int off[3] = {o5, o5 + 1024, w < 4 ? o5 + 2048 : o5};
    unsigned xbw = T_XB + w * 3328 + (((lane >> 2) & 1) * 8 + (lane >> 3)) * 208 + (lane & 3) * 16;
    unsigned xbr = T_XB + w * 3328 + i16 * 208 + g * 16;
    unsigned rdw = T_RED + (((w >> 2) * 128 + (g >> 1) * 64 + (g & 1) * 16 + i16) * 4 + (w & 3)) * 4;
    unsigned rdr = T_RED + (fb * 64 + fl) * 16;
    unsigned stw = T_WST + (((g >> 1) * 2 + (i16 & 1)) * 16 + w * 2 + (g & 1)) * 4;
    unsigned hhw = T_HH + w * 128 + lane * 2, hhr = T_HH + w * 128 + (i16 >> 3) * 64 + g * 16;
    int o6 = WS_H6 + ((fg * 2 + (g >> 1)) * 640 + 128 * w + 64 * (g & 1) + 4 * i16) * 4;
    PIN(off[0]); PIN(off[2]); PIN(idf[0]); PIN(idf[1]); PIN(xbw); PIN(xbr); PIN(rdw); PIN(rdr); PIN(stw); PIN(hhw); PIN(hhr); PIN(o6);
    PIN(x9); PIN(gb9); PIN(roc9);
#pragma unroll
    for (int s = 0; s < 3; ++s) { PIN(roc[s]); PIN(xcs[s]); PIN(cons[s][0]); PIN(cons[s][1]); }
    BAR();
    GST(14);
    wait_tag64(rs, WS_HINT5 + ((fg + (lane & 1) * 25) % 80) * 128, tlo, thi);
    GST(15);
    nap<DLY_5>();
    {
      v4u v[3];
      gather<3, 0>(rs, off, v, 0u);
      GST(16);
#pragma unroll
      for (int s = 0; s < 3; ++s) *(LAS v4u*)(L + xbw + 64 * s) = v[s];
      WAIT_LGKM0();
      f32x4 acc[4];
      float s1 = 0.f, s2 = 0.f;
#pragma unroll
      for (int s = 0; s < 3; ++s) {
        if (s < 2 || w < 4) {
          const v4u a = *(LAS v4u*)(L + xbr + 64 * s);
          const f32x4 i0 = mfma(a, idf[0], Z4), i1 = mfma(a, idf[1], Z4);
#pragma unroll
          for (int nt = 0; nt < 4; ++nt) acc[nt] = mfma(a, wf[s][nt], s == 0 ? Z4 : acc[nt]);
          const float d = merge16(hsum4(i0), hsum4(i1)) * roc[s] + xcs[s];
          s1 += d; s2 += d * d;
        }
      }
      const bool odd = i16 & 1;
      float t = (odd ? s2 : s1) + dppf<DPP_X1>(odd ? s1 : s2);
      t = dadd<DPP_ROR8>(dadd<0x124>(dadd<DPP_X2>(opq(t))));
      if (i16 < 2) *(LAS float*)(L + stw) = t;
      *(LAS float*)(L + rdw) = merge16(hsum4(acc[0]), hsum4(acc[1]));
      *(LAS float*)(L + rdw + 512) = merge16(hsum4(acc[2]), hsum4(acc[3]));
    }
    BAR();
    GST(17);
    if (w < 5) {
      const f32x4 g0 = *(LAS f32x4*)(L + rdr), g1 = *(LAS f32x4*)(L + rdr + 2048), u0 = *(LAS f32x4*)(L + rdr + 512), u1 = *(LAS f32x4*)(L + rdr + 2560);
      const float st = sum16l(wst[lane]) * (1.f / 640.f);
      auto sw = __builtin_amdgcn_permlane16_swap(asu(st), asu(st), false, false);
      const float mu = asf(sw[0]), e2 = asf(sw[1]);
      const float rsd = __builtin_amdgcn_rsqf(fmaxf(e2 - mu * mu, 0.f) + 1e-5f);
      const float gg = rsd * (hsum4(g0 + g1) + cons[2][0] - mu * cons[0][0]) + cons[1][0];
      const float uu = rsd * (hsum4(u0 + u1) + cons[2][1] - mu * cons[0][1]) + cons[1][1];
      *(LAS u16*)(L + hhw) = (u16)(pk2(uu * sigm(gg), 0.f) & 0xFFFFu);
      WAIT_LGKM0();
      const v4u a = *(LAS v4u*)(L + hhr);
      f32x4 d[8];
#pragma unroll
      for (int t = 0; t < 8; ++t) d[t] = mfma(a, wt[t], Z4);
      const bool hi = g & 1;
      const f32x4 o = {hi ? d[4].x : d[0].x, hi ? d[5].x : d[1].x, hi ? d[6].x : d[2].x, hi ? d[7].x : d[3].x};
      st16(rs, o6, tag4(o, tb));
    }
    GST(18);
    if (role9) {
      const int pp = 7 * w + (lane >> 3);
      const bool act = (lane >> 3) < 7 && pp < 54;
      int off9[1] = {WS_H6 + (((act ? pp : 0) * 2 + b9) * 640 + p9 + (lane & 7) * 4) * 4};
      unsigned am = act ? 0xFFFFFF00u : 0u;
      unsigned r9w = T_R9 + (((w >> 2) * 32 + (lane & 7) * 4 + (g & 1) * 2 + (g >> 1)) * 4 + (w & 3)) * 4;
      unsigned r9r = T_R9 + ((tid & 1) * 32 + e9) * 16;
      float x1 = 0.f;
      if (w == 0) {
        float z = 0.f;
#pragma unroll
        for (int p = 0; p < 4; ++p) {
          const unsigned hv = __builtin_amdgcn_raw_buffer_load_b16(rs, WS_H5 + ((((n9 >> 5) * 8 + (lane & 1) * 4 + p) * 2 + b9) * 32 + (n9 & 31)) * 2, 0, 16);
          z += asf(hv << 16);
        }
        x1 = x9 + dadd<DPP_X1>(z) * roc9;
      }
      PIN(off9[0]); PIN(am); PIN(r9w); PIN(r9r); PIN(x1);
      nap<DLY_6>();
      v4u v[1];
      gather<1, 1>(rs, off9, v, tb);
      GST(19);
      const float a01 = merge32(asf(v[0].x & am), asf(v[0].y & am)), a23 = merge32(asf(v[0].z & am), asf(v[0].w & am));
      const float t = dadd<DPP_ROR8>(merge16(a01, a23));
      if ((lane & 8) == 0) *(LAS float*)(L + r9w) = t;
      BAR();
      if (w == 0) {
        const float sm = dadd<DPP_X1>(hsum4(*(LAS f32x4*)(L + r9r)));
        const float o = opq(x1 + gb9 * sm);
        const unsigned ov = pk2(o, dppf<DPP_X2>(o));
        if ((lane & 3) == 0) *(GAS unsigned*)(P.out + b9 * 640 + n9) = ov;
        if (fg == 0 && tid == 0) st8(rs, WS_EPOCH, v2u{tlo, thi});
      }
      GST(20);
    }
  }
  }
  } else {
    const int ja = r - 16, hg = par, tile = (ja >> 1) * 8 + xc, b = tile >> 5, s = tile & 31;
    const int aidx = tile * 2 + hg;
    const bool roleO = ja < 10;
    const int kr = xc, ng = ja;
    const int wq4 = w & 3;
    v4u cf[32];
    unsigned mcr = LA_MISC + 8192 + i16 * 32;
    if (w < 4) {
      if (!isS1) { pace<DEP_A1>(); nap<DLY_A0>(); }
      const int rA = 32 * wq4 + 16 * hg, rB = 32 * wq4 + 16 - 16 * hg;
      const char* ck = (const char*)P.ckv + (size_t)(tile * 128) * 1024;
#pragma unroll 1
      for (int i = 0; i < 32; ++i) {
        const int row = (i < 16 ? rA : rB - 16) + i;
        dma16(ck + row * 1024, (unsigned)((lane ^ (((i & 3) << 2) | ((i >> 2) & 3))) * 16), Lb + L_TILE + row * 1024);
      }
      const unsigned sw = ((i16 & 3) << 2) | ((i16 >> 2) & 3);
      unsigned qr = LA_Q + i16 * 1040 + g * 16;
      unsigned pwA = LA_P + i16 * 272 + (rA + 4 * g) * 2, pwB = LA_P + i16 * 272 + (rB + 4 * g) * 2;
      unsigned mcw = LA_MISC + 8192 + i16 * 32 + wq4 * 4;
      int o3s = WS_H3S + (((b * 2 + hg) * 32 + s) * 16 + i16) * 16;
      PIN(qr); PIN(pwA); PIN(pwB); PIN(mcw); PIN(o3s); PIN(mcr);
      BAR();
      asm volatile("s_waitcnt vmcnt(16)" ::: "memory");
#pragma unroll
      for (int t = 0; t < 16; ++t) cf[t] = *(LAS v4u*)(L + L_TILE + (rA + i16) * 1024 + (t >> 2) * 256 + (((4 * (t & 3) + g) ^ sw) * 16));
      WAIT_VM0();
      STAMP(2);
#pragma unroll
      for (int t = 0; t < 16; ++t) cf[16 + t] = *(LAS v4u*)(L + L_TILE + (rB + i16) * 1024 + (t >> 2) * 256 + (((4 * (t & 3) + g) ^ sw) * 16));
      {
        v2u e2_ = ep; PIN(e2_);
        const unsigned elo = __builtin_amdgcn_readfirstlane(e2_.x), ehi = __builtin_amdgcn_readfirstlane(e2_.y);
        tlo = elo + 1u; thi = ehi + (tlo == 0u ? 1u : 0u); tb = tlo & 0xFFu;
      }
      WAIT_LGKM0();
      STAMP(3);
      BAR();
      STAMP(6);
      f32x4 s0 = Z4, s1 = Z4;
#pragma unroll
      for (int t = 0; t < 16; ++t) { const v4u qf = *(LAS v4u*)(L + qr + t * 64); s0 = mfma(cf[t], qf, s0); s1 = mfma(cf[16 + t], qf, s1); }
      const float mx = fmaxf(fmaxf(fmaxf(s0.x, s0.y), fmaxf(s0.z, s0.w)), fmaxf(fmaxf(s1.x, s1.y), fmaxf(s1.z, s1.w)));
      const float mc = __builtin_ceilf(maxx32(maxx16(mx)));
      float p[8];
#pragma unroll
      for (int j = 0; j < 4; ++j) { p[j] = ex2(s0[j] - mc); p[4 + j] = ex2(s1[j] - mc); }
      *(LAS v2u*)(L + pwA) = v2u{pk2(p[0], p[1]), pk2(p[2], p[3])};
      *(LAS v2u*)(L + pwB) = v2u{pk2(p[4], p[5]), pk2(p[6], p[7])};
      if (g == 0) *(LAS int*)(L + mcw) = (int)mc;
      STAMP(14);
      BAR();
      STAMP(8);
      const float ls = addx32(addx16(((p[0] + p[1]) + (p[2] + p[3])) + ((p[4] + p[5]) + (p[6] + p[7]))));
      const v4i mo = *(LAS v4i*)(L + mcr);
      const int M = imax_(imax_(mo.x, mo.y), imax_(mo.z, mo.w));
      if (g == 0) *(LAS float*)(L + mcw + 16) = __builtin_ldexpf(ls, -(int)umin_((unsigned)(M - (int)mc), 255u));
      {
        unsigned old = 0;
        WAIT_LGKM0();
        if (lane == 0) old = __hip_atomic_fetch_add((LAS unsigned*)(L + LA_MISC + 8704), 1u, __ATOMIC_RELAXED, __HIP_MEMORY_SCOPE_WORKGROUP);
        if (__builtin_amdgcn_readfirstlane(old) == 3u) {
          CFENCE();
          const f32x4 lo = *(LAS f32x4*)(L + mcr + 16);
          if (lane < 16) st16(rs, o3s, v4u{asu((float)M), tlo, asu((lo.x + lo.y) + (lo.z + lo.w)), tlo});
        }
      }
    } else {
      const int hi = r < 26 ? (r - 16) >> 1 : r - 21, mcp = 2 * hi + (xc & 1);
      const bool isM = !isS1 && hi < 10;
      if (isM) {
        const int j0 = 160 * jq + 40 * wq4 + g, jc = j0 + 4 * (i16 < 9 ? i16 : 9);
        const unsigned c0 = *(const GAS u16*)(P.c + jc), c1 = *(const GAS u16*)(P.c + 640 + jc);
        v4u wm[10];
#pragma unroll
        for (int t = 0; t < 10; ++t) wm[t] = ldnt16(P.wmod + (size_t)(j0 + 4 * t) * 3840 + 1280 + 128 * mcp + 8 * i16);
        __builtin_amdgcn_sched_barrier(0);
        const v4u sl = diag_sel(lane, i16 >> 3);
        const unsigned sp = silu_pair(c0, c1);
        f32x4 d = mfma(diag_mk(dppu<DPP_BC + 0>(sp), sl), wm[0], Z4);
        d = mfma(diag_mk(dppu<DPP_BC + 1>(sp), sl), wm[1], d); d = mfma(diag_mk(dppu<DPP_BC + 2>(sp), sl), wm[2], d);
        d = mfma(diag_mk(dppu<DPP_BC + 3>(sp), sl), wm[3], d); d = mfma(diag_mk(dppu<DPP_BC + 4>(sp), sl), wm[4], d);
        d = mfma(diag_mk(dppu<DPP_BC + 5>(sp), sl), wm[5], d); d = mfma(diag_mk(dppu<DPP_BC + 6>(sp), sl), wm[6], d);
        d = mfma(diag_mk(dppu<DPP_BC + 7>(sp), sl), wm[7], d); d = mfma(diag_mk(dppu<DPP_BC + 8>(sp), sl), wm[8], d);
        d = mfma(diag_mk(dppu<DPP_BC + 9>(sp), sl), wm[9], d);
        *(LAS f32x4*)(L + LA_MISC + (wq4 * 64 + lane) * 16) = d;
      }
      {
        const unsigned elo = __builtin_amdgcn_readfirstlane(ep.x), ehi = __builtin_amdgcn_readfirstlane(ep.y);
        tlo = elo + 1u; thi = ehi + (tlo == 0u ? 1u : 0u); tb = tlo & 0xFFu;
      }
      BAR();
      if (isM && w == 7) {
        const LAS f32x4* rp = (const LAS f32x4*)(L + LA_MISC) + lane;
        const f32x4 sm = (rp[0] + rp[64]) + (rp[128] + rp[192]);
        st16(rs, WS_MR + ((jq * 2 + (g >> 1)) * 2560 + 128 * mcp + 8 * i16 + 4 * (g & 1)) * 4, tag4(sm, tb));
      }
      PST(16);
      if (tid == 256) *(LAS unsigned*)(L + LA_MISC + 8704) = 0u;
      const int o = WS_H2 + (((b * 32 + 16 * hg + 4 * wq4 + (g >> 1)) * 2 + (g & 1)) * 512 + 8 * i16) * 2;
      int off[8] = {o, o + 256, o + 512, o + 768, o + 4096, o + 4352, o + 4608, o + 4864};
      v4u sel = diag_mk(((g >> 1) == (i16 >> 3)) ? 0x3F80u : 0u, diag_sel(lane, 0));
      unsigned qw = LA_Q + (4 * wq4 + (g >> 1)) * 1040 + (8 * i16 + 4 * (g & 1)) * 2;
      unsigned prd = LA_P + i16 * 272 + g * 16;
      int o3 = WS_H3 + ((16 * hg + 4 * g) * 4 + wq4) * 16384 + (b * 32 + s) * 256 + i16 * 16;
      PIN(off[0]); PIN(off[4]); PIN(sel); PIN(qw); PIN(prd); PIN(o3); PIN(mcr);
      nap<DLY_T>();
      wait_tag64(rs, lane < 40 ? WS_TOK + lane * 128 : WS_HINT2 + (hg * 64 + ((tile + (lane & 1) * 29) & 63)) * 128, tlo, thi);
      PST(4);
      nap<DLY_2>();
      {
        v4u v[8];
        gather<8, 0>(rs, off, v, 0u);
        PST(5);
#pragma unroll
        for (int cg = 0; cg < 8; ++cg) {
          const f32x4 d = mfma(sel, v[cg], Z4);
          *(LAS v2u*)(L + qw + (cg >> 2) * 2080 + (cg & 3) * 256) = v2u{pk2(d.x, d.y), pk2(d.z, d.w)};
        }
      }
      BAR();
      if (tid == 448) st8(rs, WS_HINT3 + aidx * 128, v2u{tlo, thi});
      {
        const unsigned q = i16 >> 2, p = lane & 3;
        const unsigned sw0 = (q << 2) | ((2 * g) & 3), sw1 = sw0 | 1u;
#pragma unroll
        for (int tt = 0; tt < 8; ++tt) {
          const unsigned chk = 2 * tt + (p >> 1);
          const unsigned a0 = L_TILE + (8 * g + q) * 1024 + wq4 * 256 + ((chk ^ sw0) * 16) + 8 * (p & 1);
          const unsigned a1 = L_TILE + (8 * g + q + 4) * 1024 + wq4 * 256 + ((chk ^ sw1) * 16) + 8 * (p & 1);
#pragma unroll
          for (int kt = 0; kt < 4; ++kt) cf[tt * 4 + kt] = trfrag(L, a0 + kt * 32768, a1 + kt * 32768);
        }
      }
      WAIT_LGKM0();
      PST(15);
      BAR();
      const v4i mo = *(LAS v4i*)(L + mcr);
      const int M = imax_(imax_(mo.x, mo.y), imax_(mo.z, mo.w));
      v4u a[4];
#pragma unroll
      for (int kt = 0; kt < 4; ++kt) {
        const v4u pf = *(LAS v4u*)(L + prd + kt * 64);
        const unsigned dl = umin_((unsigned)(M - mo[kt]), 255u);
        const u16x2 dd = __builtin_bit_cast(u16x2, dl * 0x00800080u);
#pragma unroll
        for (int j = 0; j < 4; ++j) { const unsigned e = pf[j]; a[kt][j] = __builtin_bit_cast(unsigned, __builtin_elementwise_sub_sat(__builtin_bit_cast(u16x2, e), dd)); }
      }
      v4u o4_[4];
#pragma unroll
      for (int t2 = 0; t2 < 4; ++t2) {
        f32x4 d0 = Z4, d1 = Z4;
#pragma unroll
        for (int kt = 0; kt < 4; ++kt) { d0 = mfma(a[kt], cf[(2 * t2) * 4 + kt], d0); d1 = mfma(a[kt], cf[(2 * t2 + 1) * 4 + kt], d1); }
#pragma unroll
        for (int j = 0; j < 4; ++j) o4_[j][t2] = nosent(pk2(d0[j], d1[j]));
      }
#pragma unroll
      for (int j = 0; j < 4; ++j) st16(rs, o3 + j * 65536, o4_[j]);
      PST(7);
    }
    BAR();
    if (roleO) {
    {
      const u16* wo = P.wo + (size_t)(512 * kr) * 640 + 64 * ng;
      tile_dma<8>(wo, wo + 32, 1280, 64 * w, lane, Lb + L_TILE, 0);
    }
    LAS float* MS = (LAS float*)(L + L_TILEB + 28672);
    if (w < 4) {
      int off[1] = {WS_MR + ((((tid >> 5) & 3) * 2 + ((tid >> 4) & 1)) * 2560 + (tid >> 7) * 1280 + 64 * ng + (tid & 15) * 4) * 4};
      v4u v[1];
      gather<1, 1, 0, 4>(rs, off, v, tb);
      *(LAS f32x4*)(MS + tid * 4) = untag4(v[0]);
    }
    WAIT_VM0();
    BAR();
    OST(13);
    v4u wf[3][4];
    tile_frags<2>(L, L_TILE, 64 * w, lane, wf);
    OST(9);
    const int fo = (tid >> 1) & 127;
    float scl;
    {
      const float ga = (MS[fo] + MS[128 + fo]) + (MS[256 + fo] + MS[384 + fo]);
      const float cb = (MS[512 + fo] + MS[640 + fo]) + (MS[768 + fo] + MS[896 + fo]);
      scl = ga * guard_oc(1.f + cb);
    }
    int off[1] = {WS_H4 + (8 * kr + w) * 512 + (lane & 31) * 16};
    unsigned xbw = LT_MISC + 4096 + w * 1152 + (lane >> 3) * 144 + (lane & 7) * 16;
    unsigned xbr = LT_MISC + 4096 + w * 1152 + (i16 & 3) * 144 + g * 16;
    unsigned rw = LT_MISC + (((w >> 2) * 128 + lane) * 4 + (w & 3)) * 4;
    unsigned rr = LT_MISC + ((tid & 1) * 128 + fo) * 16;
    int o5 = WS_H5 + ((((2 * ng + ((fo >> 5) & 1)) * 8 + kr) * 2 + (fo >> 6)) * 32 + (fo & 31)) * 2;
    PIN(off[0]); PIN(xbw); PIN(xbr); PIN(rw); PIN(rr); PIN(o5); PIN(scl);
    WAIT_LGKM0();
    wait_tag64(rs, WS_HINT4 + (xc * 8 + ((ng + (lane & 1) * 3) & 7)) * 128, tlo, thi);
    OST(10);
    nap<DLY_4>();
    v4u v[1];
    gather<1, 0>(rs, off, v, 0u);
    if (tid == 0) st8(rs, WS_HINT5 + (ng * 8 + kr) * 128, v2u{tlo, thi});
    OST(11);
    *(LAS v4u*)(L + xbw) = v[0];
    WAIT_LGKM0();
    const v4u a0 = *(LAS v4u*)(L + xbr), a1 = *(LAS v4u*)(L + xbr + 64);
    f32x4 acc[4];
#pragma unroll
    for (int nt = 0; nt < 4; ++nt) acc[nt] = mfma(a0, wf[0][nt], Z4);
#pragma unroll
    for (int nt = 0; nt < 4; ++nt) acc[nt] = mfma(a1, wf[1][nt], acc[nt]);
    const f32x4 e = g < 2 ? (g == 0 ? acc[0] : acc[1]) : (g == 2 ? acc[2] : acc[3]);
    *(LAS float*)(L + rw) = e.x + e.y;
    *(LAS float*)(L + rw + 1024) = e.z + e.w;
    BAR();
    if (w < 4) {
      const float z = opq(dadd<DPP_X1>(hsum4(*(LAS f32x4*)(L + rr))) * scl);
      const unsigned ov = nosent(pk2(z, dppf<DPP_X2>(z)));
      if ((lane & 3) == 0) st4(rs, o5, ov);
    }
    OST(12);
    }
  }
  WAIT_VM0();
}

extern "C" void kernel_launch(void* const* d_in, const int* in_sizes, int n_in,
                              void* d_out, int out_size, void* d_ws, size_t ws_size,
                              hipStream_t stream) {
  Params p;
  p.x = (const u16*)d_in[0]; p.ckv = (const u16*)d_in[1]; p.wq = (const u16*)d_in[2]; p.wuk = (const u16*)d_in[3];
  p.wuv = (const u16*)d_in[4]; p.wo = (const u16*)d_in[5]; p.wg = (const u16*)d_in[7]; p.wu = (const u16*)d_in[8];
  p.wd = (const u16*)d_in[9]; p.c = (const u16*)d_in[10]; p.wmod = (const u16*)d_in[11];
  p.out = (u16*)d_out; p.ws = (unsigned char*)d_ws;
  mk_kernel<<<256, 512, 0, stream>>>(p);
}
```

```cpp
#include <hip/hip_runtime.h>
#include <hip/hip_bf16.h>
#include <stdint.h>

typedef unsigned short u16;
typedef unsigned long long u64;
typedef unsigned v4u __attribute__((ext_vector_type(4)));
typedef unsigned v2u __attribute__((ext_vector_type(2)));
typedef int v4i __attribute__((ext_vector_type(4)));
typedef float f32x4 __attribute__((ext_vector_type(4)));
typedef float f32x2 __attribute__((ext_vector_type(2)));
typedef short s16x4 __attribute__((ext_vector_type(4)));
typedef unsigned short u16x2 __attribute__((ext_vector_type(2)));
typedef __bf16 bf16x8_t __attribute__((ext_vector_type(8)));
typedef __bf16 bf16x2_t __attribute__((ext_vector_type(2)));
#define GAS __attribute__((address_space(1)))
#define LAS __attribute__((address_space(3)))
#define DI __device__ __forceinline__

constexpr int WS_EPOCH = 0;
constexpr int WS_TOK   = 4096;
constexpr int WS_HINT2 = 16384;
constexpr int WS_HINT3 = 32768;
constexpr int WS_HINT4 = 49152;
constexpr int WS_HINT5 = 65536;
constexpr int WS_HINT6 = 81920;
constexpr int WS_H1    = 98304;
constexpr int WS_MR    = WS_H1 + 20480;
constexpr int WS_MC    = WS_MR + 81920;
constexpr int WS_H3S   = WS_MC + 20480;
constexpr int WS_R1    = WS_H3S + 32768;
constexpr int WS_H2    = WS_R1;
constexpr int WS_H5    = WS_H2 + 131072;
constexpr int WS_H4    = WS_H5 + 20480;
constexpr int WS_H6    = WS_R1 + 245760;
static_assert(WS_H4 + 65536 <= WS_H6, "ws map");
constexpr int WS_H3    = 1 << 20;
constexpr int WS_BYTES = WS_H3 + (2 << 20);
constexpr int WS_STAMP = 4 << 20;
constexpr int NSTAMP = 28;
static_assert(WS_H6 + 276480 <= WS_H3, "ws map");

constexpr int L_TILE = 0;
constexpr int L_TILEB = 81920;
constexpr int LT_MISC = 114688;
constexpr int LA_Q = 131072;
constexpr int LA_P = LA_Q + 16 * 1040;
constexpr int LA_MISC = LA_P + 16 * 272;
constexpr int L_STAMP = 163840 - 256;
constexpr int LDS_BYTES = 163840;
static_assert(LA_MISC + 9216 + 1024 <= L_STAMP, "lds map");

#define DLY_P0 0
#define DLY_A0 0
#define DLY_2 0
#define DLY_3 6
#define DLY_4 0
#define DLY_5 0
#define DLY_6 0
#define NHELP 6
#define NWOW 10
#define RETRY_SLP 0
#define DLY_G 90
#define DLY_B 0
#define FRONT 0
#define CODE_WARM_R 99
#define ORDER 0
#define DLY_T 0
#define NBAND 0
#define DLY_3S 10
#define PV_PRE 5
#define NB2 0
#define DLY_C 50
#define H4LATE 1
#define H2LATE 1
#define H6HINT 1
#define H3ALL 1
#define NWO 0
#define WO_TOUCH 1
#define DEP_P1 63
#define DEP_A1 0
#define DEP_2 63
template <int D> __device__ __forceinline__ void pace() { if (D < 63) asm volatile("s_waitcnt vmcnt(%0)" :: "i"(D) : "memory"); }
constexpr int SPIN_CAP = 1 << 18;
constexpr unsigned SENT = 0xFFFFFFFFu;

struct Params {
  const u16 *x, *ckv, *wq, *wuk, *wuv, *wo, *wg, *wu, *wd, *c, *wmod;
  u16* out;
  unsigned char* ws;
};

DI float asf(unsigned u) { return __builtin_bit_cast(float, u); }
DI unsigned asu(float f) { return __builtin_bit_cast(unsigned, f); }
DI unsigned pk2(float a, float b) { f32x2 v = {a, b}; return __builtin_bit_cast(unsigned, __builtin_convertvector(v, bf16x2_t)); }
DI float blo(unsigned w) { return asf(w << 16); }
DI float bhi(unsigned w) { return asf(w & 0xFFFF0000u); }
DI f32x4 mfma(v4u a, v4u b, f32x4 c) { return __builtin_amdgcn_mfma_f32_16x16x32_bf16(__builtin_bit_cast(bf16x8_t, a), __builtin_bit_cast(bf16x8_t, b), c, 0, 0, 0); }
DI float opq(float x) { asm("" : "+v"(x)); return x; }
#define PIN(x) asm volatile("" : "+v"(x))
template <int CTRL> DI unsigned dppu(unsigned v) { return (unsigned)__builtin_amdgcn_update_dpp(0, (int)v, CTRL, 0xf, 0xf, true); }
template <int CTRL> DI float dppf(float v) { return asf(dppu<CTRL>(asu(v))); }
template <int CTRL> DI float dadd(float v) { return opq(v + dppf<CTRL>(v)); }
constexpr int DPP_X1 = 0xB1, DPP_X2 = 0x4E, DPP_HMIR = 0x141, DPP_MIR = 0x140, DPP_ROR8 = 0x128, DPP_BC = 0x150;
DI float sum16l(float v) { return dadd<DPP_MIR>(dadd<DPP_HMIR>(dadd<DPP_X2>(dadd<DPP_X1>(v)))); }
DI float merge16(float a, float b) { auto s = __builtin_amdgcn_permlane16_swap(asu(a), asu(b), false, false); return opq(asf(s[0]) + asf(s[1])); }
DI float merge32(float a, float b) { auto s = __builtin_amdgcn_permlane32_swap(asu(a), asu(b), false, false); return opq(asf(s[0]) + asf(s[1])); }
DI float addx16(float v) { return merge16(v, v); }
DI float addx32(float v) { return merge32(v, v); }
DI float maxx16(float v) { auto s = __builtin_amdgcn_permlane16_swap(asu(v), asu(v), false, false); return fmaxf(asf(s[0]), asf(s[1])); }
DI float maxx32(float v) { auto s = __builtin_amdgcn_permlane32_swap(asu(v), asu(v), false, false); return fmaxf(asf(s[0]), asf(s[1])); }
DI float hsum4(f32x4 a) { const f32x2 t = f32x2{a.x, a.y} + f32x2{a.z, a.w}; return opq(t.x + t.y); }
DI float ex2(float v) { return __builtin_amdgcn_exp2f(v); }
DI float rcp(float v) { return __builtin_amdgcn_rcpf(v); }
DI float sigm(float v) { return rcp(1.f + ex2(-1.4426950408889634f * v)); }
#define WAIT_VM0() asm volatile("s_waitcnt vmcnt(0)" ::: "memory")
#define WAIT_LGKM0() asm volatile("s_waitcnt lgkmcnt(0)" ::: "memory")
#define BAR() asm volatile("s_waitcnt lgkmcnt(0)\n\ts_barrier" ::: "memory")
#define CFENCE() asm volatile("" ::: "memory")
template <int N> DI void nap() {
#pragma unroll
  for (int i = 0; i < N / 64; ++i) __builtin_amdgcn_s_sleep(64);
  if (N % 64) __builtin_amdgcn_s_sleep(N % 64);
}
DI void dma16(const void* base, unsigned voff, unsigned ldsaddr) {
  unsigned keep;
  asm volatile("s_mov_b32 %0, m0\n\ts_mov_b32 m0, %3\n\ts_nop 0\n\tglobal_load_lds_dwordx4 %1, %2 nt\n\ts_mov_b32 m0, %0"
               : "=&s"(keep) : "v"(voff), "s"(base), "s"(ldsaddr) : "memory");
}
#define WARM_POL "nt"
DI void warm16(const void* base, unsigned voff, unsigned ldsaddr) {
  unsigned keep;
  asm volatile("s_mov_b32 %0, m0\n\ts_mov_b32 m0, %3\n\ts_nop 0\n\tglobal_load_lds_dwordx4 %1, %2 " WARM_POL "\n\ts_mov_b32 m0, %0"
               : "=&s"(keep) : "v"(voff), "s"(base), "s"(ldsaddr) : "memory");
}
DI void dma16v(const void* lane_ptr, unsigned ldsaddr) {
  unsigned keep;
  asm volatile("s_mov_b32 %0, m0\n\ts_mov_b32 m0, %2\n\ts_nop 0\n\tglobal_load_lds_dwordx4 %1, off nt\n\ts_mov_b32 m0, %0"
               : "=&s"(keep) : "v"(lane_ptr), "s"(ldsaddr) : "memory");
}
typedef __amdgpu_buffer_rsrc_t rsrc_t;
DI v4u ld16(rsrc_t rs, int off) { return __builtin_amdgcn_raw_buffer_load_b128(rs, off, 0, 16); }
DI v2u ld8(rsrc_t rs, int off) { return __builtin_amdgcn_raw_buffer_load_b64(rs, off, 0, 16); }
DI void st16(rsrc_t rs, int off, v4u v) { __builtin_amdgcn_raw_buffer_store_b128(v, rs, off, 0, 16); }
DI void st8(rsrc_t rs, int off, v2u v) { __builtin_amdgcn_raw_buffer_store_b64(v, rs, off, 0, 16); }
DI void st4(rsrc_t rs, int off, unsigned v) { __builtin_amdgcn_raw_buffer_store_b32(v, rs, off, 0, 16); }
DI v4u ldnt16(const void* p) { return __builtin_nontemporal_load((const GAS v4u*)p); }
DI unsigned umin_(unsigned a, unsigned b) { return a < b ? a : b; }
DI unsigned umax_(unsigned a, unsigned b) { return a > b ? a : b; }
DI int imax_(int a, int b) { return a > b ? a : b; }
DI unsigned nosent(unsigned w) { return umin_(w, 0xFFFFFFFEu); }
DI unsigned tagf(float f, unsigned tb) { return (asu(f) & 0xFFFFFF00u) | tb; }
DI v4u tag4(f32x4 f, unsigned tb) { return v4u{tagf(f.x, tb), tagf(f.y, tb), tagf(f.z, tb), tagf(f.w, tb)}; }
DI f32x4 untag4(v4u v) { return f32x4{asf(v.x & 0xFFFFFF00u), asf(v.y & 0xFFFFFF00u), asf(v.z & 0xFFFFFF00u), asf(v.w & 0xFFFFFF00u)}; }
DI unsigned bad0(v4u v) { return umax_(umax_(v.x, v.y), umax_(v.z, v.w)); }
DI unsigned bad1(v4u v, unsigned tb) { return __builtin_amdgcn_bitop3_b32(v.x, v.y, tb, 0x7E) | __builtin_amdgcn_bitop3_b32(v.z, v.w, tb, 0x7E); }
DI unsigned bad2(v4u v, unsigned t) { return __builtin_amdgcn_bitop3_b32(v.y, v.w, t, 0x7E); }

template <int N, int MODE0, int NB = 0, int SLP = RETRY_SLP>
DI void gather(rsrc_t rs, const int (&off)[N + NB], v4u (&v)[N + NB], unsigned t0, unsigned t1 = 0) {
#pragma unroll 1
  for (int spins = 0; spins < SPIN_CAP; ++spins) {
    CFENCE();
#pragma unroll
    for (int i = 0; i < N + NB; ++i) v[i] = ld16(rs, off[i]);
    unsigned a = 0, b = 0;
#pragma unroll
    for (int i = 0; i < N; ++i) a = MODE0 == 0 ? umax_(a, bad0(v[i])) : (a | bad1(v[i], t0));
#pragma unroll
    for (int i = N; i < N + NB; ++i) b |= bad2(v[i], t1);
    const bool ok = (MODE0 == 0 ? a != SENT : (a & 0xFFu) == 0u) && b == 0u;
    if (__all(ok)) break;
    if (SLP) __builtin_amdgcn_s_sleep(SLP);
  }
  CFENCE();
}
DI void wait_tag64(rsrc_t rs, int off, unsigned tlo, unsigned thi) {
#pragma unroll 1
  for (int spins = 0; spins < SPIN_CAP; ++spins) {
    CFENCE();
    const v2u v = ld8(rs, off);
    if (__all(v.x == tlo && v.y == thi)) break;
    __builtin_amdgcn_s_sleep(1);
  }
  CFENCE();
}
DI v4u trfrag(LAS unsigned char* L, unsigned a0, unsigned a1) {
  const s16x4 x = __builtin_amdgcn_ds_read_tr16_b64_v4i16((LAS s16x4*)(L + a0));
  const s16x4 y = __builtin_amdgcn_ds_read_tr16_b64_v4i16((LAS s16x4*)(L + a1));
  const v2u xx = __builtin_bit_cast(v2u, x), yy = __builtin_bit_cast(v2u, y);
  return v4u{xx.x, xx.y, yy.x, yy.y};
}
DI v4u diag_sel(int lane, unsigned hsel) {
  const unsigned two = ((2 * hsel + 1) << 8) | (2 * hsel);
  const unsigned s = (lane & 1) ? ((two << 16) | 0x0C0Cu) : (0x0C0C0000u | two);
  const int d = (lane & 7) >> 1;
  return v4u{d == 0 ? s : 0x0C0C0C0Cu, d == 1 ? s : 0x0C0C0C0Cu, d == 2 ? s : 0x0C0C0C0Cu, d == 3 ? s : 0x0C0C0C0Cu};
}
DI v4u diag_mk(unsigned v, v4u sel) {
  return v4u{__builtin_amdgcn_perm(0u, v, sel.x), __builtin_amdgcn_perm(0u, v, sel.y), __builtin_amdgcn_perm(0u, v, sel.z), __builtin_amdgcn_perm(0u, v, sel.w)};
}
DI v4u and4(v4u a, unsigned m) { return v4u{a.x & m, a.y & m, a.z & m, a.w & m}; }

#define STAMP(k) do { } while (0)
#define STAMPX(k) do { } while (0)

#define OST(k) STAMP(k)
#define GST(k) STAMP(k)
#define GSX(k) STAMPX(k)
#define S1ST(k)
#define PST(k)
template <int NI, bool TWO = false, int DEP = 63>
DI void tile_dma(const void* baseL, const void* baseR, unsigned stride, int k0, int lane, unsigned ldsbase, int rot) {
  const unsigned r8 = lane >> 3, cpos = lane & 7;
  const unsigned chE = (((cpos >> 1) ^ ((r8 >> 1) & 1u)) << 1) | (cpos & 1), chO = chE ^ 4u;
  if (TWO) {
    const char* pE = (const char*)((chE >> 2) ? baseR : baseL) + (r8 * stride + (chE & 3) * 16);
    const char* pO = (const char*)((chO >> 2) ? baseR : baseL) + (r8 * stride + (chO & 3) * 16);
#pragma unroll
    for (int i = 0; i < NI; ++i) {
      int ii = i + rot; if (ii >= NI) ii -= NI;
      dma16v(((i & 1) ? pO : pE) + (size_t)((unsigned)(k0 + 8 * ii) * stride), ldsbase + (unsigned)(k0 + 8 * ii) * 128u);
      pace<DEP>();
    }
  } else {
    const unsigned vE = r8 * stride + chE * 16, vO = r8 * stride + chO * 16;
#pragma unroll
    for (int i = 0; i < NI; ++i) {
      int ii = i + rot; if (ii >= NI) ii -= NI;
      dma16((const char*)baseL + (size_t)((unsigned)(k0 + 8 * ii) * stride), (i & 1) ? vO : vE, ldsbase + (unsigned)(k0 + 8 * ii) * 128u);
      pace<DEP>();
    }
  }
}
template <int NS>
DI void tile_frags(LAS unsigned char* L, unsigned ldsoff, int k0, int lane, v4u (&w)[3][4]) {
  const unsigned g = lane >> 4, q = (lane & 15) >> 2, p = lane & 3;
  const unsigned f = (q >> 1) | ((g & 1) << 1);
  const unsigned base = ldsoff + (k0 + 8 * g + q) * 128 + 8 * p;
#pragma unroll
  for (int s = 0; s < NS; ++s)
#pragma unroll
    for (int nt = 0; nt < 4; ++nt) {
      const unsigned a = base + s * 4096 + ((nt ^ f) * 32);
      w[s][nt] = trfrag(L, a, a + 512);
    }
}
struct Row { v4u a, b; };
DI Row row_load(const u16* row, int lane) {
  Row r; r.a = *(const GAS v4u*)(row + lane * 8); r.b = *(const GAS v4u*)(row + 512 + (lane & 15) * 8); return r;
}
DI float wave_sum(float v) { return addx32(addx16(sum16l(v))); }
DI float sum8bf(v4u v) { return ((blo(v.x) + bhi(v.x)) + (blo(v.y) + bhi(v.y))) + ((blo(v.z) + bhi(v.z)) + (blo(v.w) + bhi(v.w))); }
DI float dev8bf(v4u v, float mu) {
  float s = 0.f;
#pragma unroll
  for (int j = 0; j < 4; ++j) { const unsigned e = v[j]; const float a = blo(e) - mu, b = bhi(e) - mu; s += a * a + b * b; }
  return s;
}
DI float row_mean(const Row& r, int lane) { return wave_sum(sum8bf(r.a) + (lane < 16 ? sum8bf(r.b) : 0.f)) * (1.f / 640.f); }
DI float row_var(const Row& r, int lane, float mu) { return wave_sum(dev8bf(r.a, mu) + (lane < 16 ? dev8bf(r.b, mu) : 0.f)) * (1.f / 640.f); }
DI float guard_oc(float oc) { return fabsf(oc) < 1e-18f ? 1e-18f : oc; }

DI unsigned silu_pair(unsigned c0, unsigned c1) { const float a = asf(c0 << 16), b = asf(c1 << 16); return pk2(a * sigm(a), b * sigm(b)); }
DI f32x4 mod_mfma(unsigned sp, v4u sel, const v4u (&wm)[5]) {
  f32x4 d = mfma(diag_mk(dppu<DPP_BC + 0>(sp), sel), wm[0], f32x4{0.f, 0.f, 0.f, 0.f});
  d = mfma(diag_mk(dppu<DPP_BC + 1>(sp), sel), wm[1], d);
  d = mfma(diag_mk(dppu<DPP_BC + 2>(sp), sel), wm[2], d);
  d = mfma(diag_mk(dppu<DPP_BC + 3>(sp), sel), wm[3], d);
  return mfma(diag_mk(dppu<DPP_BC + 4>(sp), sel), wm[4], d);
}
#define CW_STR 64
constexpr int CODE_WARM_LINES = 24064 / CW_STR;

__global__ void __launch_bounds__(512, 2) mk_kernel(Params P) {
  __shared__ __attribute__((aligned(16))) unsigned char smem[LDS_BYTES];
  LAS unsigned char* L = (LAS unsigned char*)smem;
  const unsigned Lb = (unsigned)(size_t)L;
  const int tid = threadIdx.x, lane = tid & 63;
  const int w = __builtin_amdgcn_readfirstlane(tid >> 6);
  const int bid = blockIdx.x, xc = bid & 7, r = bid >> 3;
  const int g = lane >> 4, i16 = lane & 15;
  if (w < (CODE_WARM_LINES + 63) / 64 && r >= CODE_WARM_R) {
    const u64 pc = __builtin_amdgcn_s_getpc() & ~127ull;
    unsigned keep;
    asm volatile("s_mov_b32 %0, m0\n\ts_mov_b32 m0, %3\n\ts_nop 0\n\tglobal_load_lds_dword %1, %2\n\ts_mov_b32 m0, %0"
                 : "=&s"(keep) : "v"((tid < CODE_WARM_LINES ? tid : 0) * CW_STR), "s"(pc), "s"(Lb + L_STAMP - 1024) : "memory");
  }
  const bool isS1 = (unsigned)(r - 16) < 10u && !(r & 1);
  const int k0w = w < 4 ? 96 * w : 128 + 64 * w;
  const int slot = ((r - 16) >> 1) * 8 + xc, cgp = ((r - 16) & 14) + (xc & 1), jq = xc >> 1;
  const int mfb = w >> 2, mfm = 16 * (w & 3) + (lane >> 2), mty = (lane >> 1) & 1;
  unsigned xs0, xs1, xs2, xs3, xs4, xm, c0s, c1s;
  const rsrc_t rs = __builtin_amdgcn_make_buffer_rsrc((void*)P.ws, 0, WS_BYTES, 0x00020000);
  v2u ep;
  {
    const bool isP = r < 16;
    const int hi = r < 26 ? (r - 16) >> 1 : r - 21;
    unsigned lS = w * 5120, lP = k0w * 128;
    unsigned oS = (8 * jq + w) * 153600 + 128 * cgp, oP = lP * 64 + xc * 1024 + (r >> 1) * 128, oA = (hi * 8 + w) * 8192 + xc * 1024;
    asm("" : "+s"(lS), "+s"(lP), "+s"(oS), "+s"(oP), "+s"(oA));
    const unsigned off = isS1 ? oS : isP ? oP : oA;
    unsigned lds = Lb + (isS1 ? lS : isP ? lP : (unsigned)(L_STAMP - 1024));
    const unsigned lstep = (isS1 || isP) ? 1024u : 0u;
    const unsigned step = isS1 ? 30720u : isP ? 65536u : 88u * 8192u;
    const int cnt = isS1 ? 5 : isP ? (w < 4 ? 12 : 8) : ((hi < 9 || NHELP < 6) ? NHELP : NHELP - 1);
    const unsigned xmask = isP ? 0x40u : 0u;
    unsigned vS = g * 7680u + (i16 >> 3) * 1280u + (i16 & 7) * 16u;
    unsigned vP = (lane >> 3) * 8192u + (((lane & 7) ^ (g & 1) * 2) * 16u);
    unsigned vA = lane * 16u;
    unsigned vx = mfb * 1280 + lane * 4, vm = (mfb * 640 + 64 * cgp + mfm) * 2, vc = (160 * jq + 20 * w + g + 4 * (i16 < 4 ? i16 : 4)) * 2;
    unsigned vz = (isS1 && w >= 2) ? WS_R1 + slot * 6144 + (tid - 128) * 16 : WS_BYTES;
    asm("" : "+v"(vS), "+v"(vP), "+v"(vA));
    const unsigned vb = isS1 ? vS : isP ? vP : vA;
    if (DLY_P0 + DLY_B > 0 && !isS1) { if (isP) nap<DLY_P0>(); else nap<DLY_B>(); }
    asm volatile("" :: "s"(P.x), "s"(P.ckv), "s"(P.wq), "s"(P.wuk), "s"(P.c), "s"(P.wmod), "s"(P.ws), "s"(off), "s"(lds), "s"(step), "s"(cnt), "s"(xmask), "s"(lstep), "v"(vb), "v"(vx), "v"(vm), "v"(vc), "v"(vz));
    if (isS1) {
      asm volatile("global_load_ushort %6, %10, %12\n\tglobal_load_ushort %7, %10, %12 offset:1280\n\t"
                   "global_load_dword %0, %8, %11\n\tglobal_load_dword %1, %8, %11 offset:256\n\tglobal_load_dword %2, %8, %11 offset:512\n\t"
                   "global_load_dword %3, %8, %11 offset:768\n\tglobal_load_dword %4, %8, %11 offset:1024\n\tglobal_load_ushort %5, %9, %11"
                   : "=&v"(xs0), "=&v"(xs1), "=&v"(xs2), "=&v"(xs3), "=&v"(xs4), "=&v"(xm), "=&v"(c0s), "=&v"(c1s)
                   : "v"(vx), "v"(vm), "v"(vc), "s"(P.x), "s"(P.c) : "memory");
    }
    if (!isS1 && !isP && hi < 10) {
      const unsigned sc = (3u - ((unsigned)(size_t)P.wq >> 7)) & 7u;
      dma16((const char*)P.wq + ((hi * 8 + w) * 65536 + xc * 1024 + sc * 128), vP, lds);
    }
    const char* base = (const char*)(isS1 ? P.wmod : isP ? P.wq : P.wuk) + off;
#pragma unroll 1
    for (int i = 0; i < cnt; ++i) {
      dma16(base, vb ^ ((i << 6) & xmask), lds);
      base += step; lds += lstep;
    }
    ep = ld8(rs, WS_EPOCH);
    if (isS1) st16(rs, vz, v4u{SENT, SENT, SENT, SENT});
    CFENCE();
  }
  S1ST(21);
  if (isS1) {
    v4u sel = diag_sel(lane, i16 >> 3);
    unsigned rw = LA_MISC + ((w >> 2) * 256 + lane) * 16 + (w & 3) * 4;
    unsigned rr = LA_MISC + ((lane & 1) * 256 + (mfm & 3) * 64 + (2 * mfb + ((mfm >> 2) & 1)) * 16 + (mfm >> 3) + 8 * mty) * 16;
    int o1 = WS_H1 + ((((2 * cgp + (mfm >> 5)) * 2 + mfb) * 4 + jq) * 32 + (mfm & 31)) * 4;
    PIN(sel); PIN(rw); PIN(rr); PIN(o1);
    S1ST(17);
    asm volatile("s_waitcnt vmcnt(7)" : "+v"(xs0), "+v"(xs1), "+v"(xs2), "+v"(xs3), "+v"(xs4), "+v"(xm), "+v"(c0s), "+v"(c1s) :: "memory");
    unsigned sp = silu_pair(c0s, c1s);
    const f32x2 e0 = {blo(xs0), bhi(xs0)}, e1 = {blo(xs1), bhi(xs1)}, e2 = {blo(xs2), bhi(xs2)}, e3 = {blo(xs3), bhi(xs3)}, e4 = {blo(xs4), bhi(xs4)};
    const f32x2 es = ((e0 + e1) + (e2 + e3)) + e4;
    const float mu = wave_sum(es.x + es.y) * (1.f / 640.f);
    const f32x2 d0 = e0 - mu, d1 = e1 - mu, d2 = e2 - mu, d3 = e3 - mu, d4 = e4 - mu;
    const f32x2 q2 = d0 * d0 + d1 * d1 + d2 * d2 + d3 * d3 + d4 * d4;
    const float rsd = __builtin_amdgcn_rsqf(wave_sum(q2.x + q2.y) * (1.f / 640.f) + 1e-5f);
    float xn = (asf(xm << 16) - mu) * rsd;
    PIN(sp); PIN(xn);
    S1ST(18);
    WAIT_VM0();
    v4u wm[5];
#pragma unroll
    for (int t = 0; t < 5; ++t) wm[t] = *(LAS v4u*)(L + (w * 5 + t) * 1024 + lane * 16);
    const f32x4 d = mod_mfma(sp, sel, wm);
    v2u e2_ = ep; PIN(e2_);
    const unsigned elo = __builtin_amdgcn_readfirstlane(e2_.x), ehi = __builtin_amdgcn_readfirstlane(e2_.y);
    const unsigned tl = elo + 1u, th = ehi + (tl == 0u ? 1u : 0u);
#pragma unroll
    for (int j = 0; j < 4; ++j) *(LAS float*)(L + rw + j * 1024) = d[j];
    S1ST(19);
    BAR();
    S1ST(20);
    const float v = dadd<DPP_X1>(hsum4(*(LAS f32x4*)(L + rr)));
    const float hp = xn * (dppf<DPP_X2>(v) + (jq == 0 ? 1.f : 0.f)) + v;
    if ((lane & 3) == 0) st4(rs, o1, pk2(0.f, hp) | (tl & 0xFFu));
    if (tid == 1) st8(rs, WS_TOK + slot * 128, v2u{tl, th});
    STAMP(1);
  }
  const v4u SENT4 = v4u{SENT, SENT, SENT, SENT};
  const f32x4 Z4 = {0.f, 0.f, 0.f, 0.f};
  const int par = r & 1;
  const unsigned Ldump = Lb + L_STAMP - 1024;
  unsigned tlo, thi, tb;

  if (r < 16) {
    const int h = 4 * xc + (r >> 2), sub = r & 3, dh = sub >> 1, ch = sub & 1, hg = xc >> 2;
    const int pidx = xc * 16 + r;
    {
      const u16* wk = P.wuk + h * 128 + dh * 64 + (size_t)(256 * ch + 32 * w) * 4096;
#pragma unroll
      for (int i = 0; i < 4; ++i) {
        const unsigned lr = 8 * i + (lane >> 3);
        dma16(wk, lr * 8192u + (((lane & 7) ^ ((lr >> 1) & 7u)) * 16), Lb + L_TILEB + w * 4096 + i * 1024);
      }
      const int c3 = WS_H3 + (h * 4 + sub) * 16384 + tid * 16;
      st16(rs, c3, SENT4); st16(rs, c3 + 8192, SENT4);
    }
    const int nl = xc < 3 ? 4 : 3, lc0 = 3 * xc + (xc < 3 ? xc : 3);
    const int fg = 2 * (lc0 + (r >> 2)) + par;
    WAIT_VM0();
    {
      v2u e2_ = ep; PIN(e2_);
      const unsigned elo = __builtin_amdgcn_readfirstlane(e2_.x), ehi = __builtin_amdgcn_readfirstlane(e2_.y);
      tlo = elo + 1u; thi = ehi + (tlo == 0u ? 1u : 0u); tb = tlo & 0xFFu;
    }
    STAMP(1); STAMPX(9);
    {
      const unsigned cl = (lane & 31) < 8 * nl ? (lane & 31) : 0;
      const char* gsrc = (const char*)(((w & 1) ? P.wu : P.wg) + 64 * lc0 + (size_t)(40 * r + 10 * (w >> 1)) * 1728);
#pragma unroll 1
      for (int i = 0; i < NB2; ++i) warm16(gsrc + i * 6912, (lane >> 5) * 3456u + cl * 16u, Ldump);
    }
    {
      v4u wf[3][4];
      v4u uk[2][2];
      if (w < 4) tile_frags<3>(L, L_TILE, k0w, lane, wf);
      else       tile_frags<2>(L, L_TILE, k0w, lane, wf);
#pragma unroll
      for (int u = 0; u < 2; ++u)
#pragma unroll
        for (int t = 0; t < 2; ++t) uk[u][t] = *(LAS v4u*)(L + L_TILEB + w * 4096 + (2 * i16 + u) * 128 + (((4 * t + g) ^ (i16 & 7)) * 16));
      const int o = WS_H1 + (k0w >> 5) * 1024 + lane * 16;
      int off[3] = {o, o + 1024, w < 4 ? o + 2048 : o};
      unsigned xbw = LT_MISC + 4096 + w * 1664 + (lane >> 3) * 208 + (lane & 7) * 8;
      unsigned xbr = LT_MISC + 4096 + w * 1664 + (i16 & 7) * 208 + g * 16;
      unsigned qxw = LT_MISC + (8 * (g & 1) + w) * 144 + ((g >> 1) * 32 + i16) * 2;
      unsigned qxr = LT_MISC + i16 * 144 + g * 16;
      int o2 = WS_H2 + ((((g >> 1) * 32 + h) * 2 + dh) * 512 + 256 * ch + 32 * w + 2 * i16) * 2;
      PIN(off[0]); PIN(off[2]); PIN(xbw); PIN(xbr); PIN(qxw); PIN(qxr); PIN(o2);
      WAIT_LGKM0();
      STAMP(2);
      v4u v[3];
      gather<3, 1>(rs, off, v, tb);
      STAMP(3); STAMPX(10);
#pragma unroll
      for (int s = 0; s < 3; ++s) *(LAS v2u*)(L + xbw + 64 * s) = v2u{__builtin_amdgcn_perm(v[s].y, v[s].x, 0x07060302u), __builtin_amdgcn_perm(v[s].w, v[s].z, 0x07060302u)};
      WAIT_LGKM0();
      f32x4 acc[4];
      v4u xa[3];
#pragma unroll
      for (int s = 0; s < 3; ++s) xa[s] = *(LAS v4u*)(L + xbr + 64 * s);
      __builtin_amdgcn_sched_barrier(0);
#pragma unroll
      for (int s = 0; s < 3; ++s) {
        if (s < 2 || w < 4) {
#pragma unroll
          for (int nt = 0; nt < 4; ++nt) acc[nt] = mfma(xa[s], wf[s][nt], s == 0 ? Z4 : acc[nt]);
        }
      }
      const float sc = 0.08838834764831845f * 1.4426950408889634f;
      const float h0 = hsum4(acc[0]), h1 = hsum4(acc[1]), h2 = hsum4(acc[2]), h3 = hsum4(acc[3]);
      const unsigned pe = pk2((g < 2 ? h0 : h2) * sc, (g < 2 ? h1 : h3) * sc);
      *(LAS u16*)(L + qxw) = (u16)(pe & 0xFFFFu); *(LAS u16*)(L + qxw + 32) = (u16)(pe >> 16);
      BAR();
      if (tid == 448) st8(rs, WS_HINT2 + pidx * 128, v2u{tlo, thi});
      STAMP(7);
      const v4u qa0 = *(LAS v4u*)(L + qxr), qa1 = *(LAS v4u*)(L + qxr + 64);
      f32x4 d0 = mfma(qa0, uk[0][0], Z4), d1 = mfma(qa0, uk[1][0], Z4);
      d0 = mfma(qa1, uk[0][1], d0); d1 = mfma(qa1, uk[1][1], d1);
      const unsigned ov = nosent(pk2(addx16(hsum4(d0)), addx16(hsum4(d1))));
      if ((g & 1) == 0) st4(rs, o2, ov);
    }
    STAMP(4);
    if (sub < 2) {
      {
        const u16* wv = P.wuv + (size_t)(256 * sub) * 4096 + h * 128;
        tile_dma<4>(wv, wv + 32, 8192, 32 * w, lane, Lb + L_TILE, 0);
        tile_dma<4>(wv + 64, wv + 96, 8192, 32 * w, lane, Lb + L_TILE + 32768, 0);
      }
      const char* wos = (const char*)P.wo + (size_t)(xc + 1) * 655360 - (size_t)((((r >> 2) * 2 + sub) * 8 + w + 1) * NWO) * 1024;
#pragma unroll
      for (int i = 0; i < NWO; ++i) warm16(wos + i * 1024, lane * 16u, Ldump);
      pace<NWO>();
      BAR();
      STAMP(5);
      v4u uv[8];
      {
        const unsigned q = i16 >> 2, p = lane & 3;
#pragma unroll
        for (int t = 0; t < 8; ++t) {
          const unsigned f = (g & 1) | ((t & 1) << 1);
          const unsigned a = L_TILE + (w >> 2) * 32768 + (64 * (t >> 1) + 16 * q + 8 * (t & 1) + 2 * g) * 128 + 8 * p + (((w & 3) ^ f) * 32);
          uv[t] = trfrag(L, a, a + 128);
        }
      }
      const int b = w >> 2, sq = w & 3;
      const int od = WS_H3 + (h * 4 + 2 * sub) * 16384 + (b * 32 + 8 * sq) * 256 + lane * 16;
      const int os = WS_H3S + (((b * 2 + hg) * 32 + 8 * sq + g) * 16 + (h & 15)) * 16;
      int off[6] = {od, od + 1024, od + 16384, od + 17408, os, os + 1024};
      v4u sl0 = diag_sel(lane, 0), sl1 = diag_sel(lane, 1);
      unsigned x5w = LT_MISC + 4096 + w * 528 + (g >> 1) * 256 + (8 * i16 + 4 * (g & 1)) * 2;
      unsigned x5r = LT_MISC + 4096 + (i16 & 7) * 528 + g * 16;
      unsigned stw = LT_MISC + 8704 + b * 32 + sq * 4, str = LT_MISC + 8704 + (g & 1) * 32;
      int o4 = WS_H4 + ((((h * 2 + (w >> 2)) * 2 + (g & 1)) * 2 + sub) * 64 + 16 * (w & 3) + i16) * 2;
      PIN(off[0]); PIN(off[2]); PIN(off[4]); PIN(sl0); PIN(sl1); PIN(x5w); PIN(x5r); PIN(stw); PIN(str); PIN(o4);
      if (NWO && WO_TOUCH) {
        WAIT_VM0();
        unsigned keep;
        asm volatile("s_mov_b32 %0, m0\n\ts_mov_b32 m0, %3\n\ts_nop 0\n\tglobal_load_lds_dword %1, %2\n\ts_mov_b32 m0, %0"
                     : "=&s"(keep) : "v"((lane < 8 * NWO ? lane : 0) * 128), "s"(wos), "s"(Ldump) : "memory");
      }
      WAIT_LGKM0();
      STAMP(25);
      wait_tag64(rs, WS_HINT3 + ((((pidx * 2 + (lane & 1) * 33) & 63) * 2) + hg) * 128, tlo, thi);
      STAMP(26);
      wait_tag64(rs, WS_HINT3 + 64 + ((b * 32 + 8 * sq + (lane & 7)) * 2 + hg) * 128, tlo, thi);
      STAMP(23);
      nap<DLY_3>();
      v4u v[6];
      gather<4, 0, 2>(rs, off, v, 0u, tlo);
      STAMP(11);
      int mw = imax_((int)v[4].x, (int)v[5].x);
      { auto sw = __builtin_amdgcn_permlane16_swap((unsigned)mw, (unsigned)mw, false, false); mw = imax_((int)sw[0], (int)sw[1]); }
      { auto sw = __builtin_amdgcn_permlane32_swap((unsigned)mw, (unsigned)mw, false, false); mw = imax_((int)sw[0], (int)sw[1]); }
      const unsigned e0 = 127u - umin_((unsigned)(mw - (int)v[4].x), 127u), e1 = 127u - umin_((unsigned)(mw - (int)v[5].x), 127u);
      const unsigned pe = (e0 << 7) | (e1 << 23);
      const v4u a0 = diag_mk(pe, sl0), a1 = diag_mk(pe, sl1);
      f32x4 d0 = mfma(a0, v[0], Z4), d1 = mfma(a0, v[2], Z4);
      d0 = mfma(a1, v[1], d0); d1 = mfma(a1, v[3], d1);
      const float lw = addx32(addx16(asf(e0 << 23) * asf(v[4].z) + asf(e1 << 23) * asf(v[5].z)));
      *(LAS int*)(L + stw) = mw; *(LAS float*)(L + stw + 16) = lw;
      const f32x4 d = g < 2 ? d0 : d1;
      *(LAS v2u*)(L + x5w) = v2u{pk2(d.x, d.y), pk2(d.z, d.w)};
      BAR();
      if (tid == 448) st8(rs, WS_HINT4 + (h * 2 + sub) * 128, v2u{tlo, thi});
      STAMP(12);
      const v4i ms = *(LAS v4i*)(L + str);
      const f32x4 ls = *(LAS f32x4*)(L + str + 16);
      v4u xa[8];
#pragma unroll
      for (int t = 0; t < 8; ++t) xa[t] = *(LAS v4u*)(L + x5r + t * 64);
      __builtin_amdgcn_sched_barrier(0);
      f32x4 q0 = Z4, q1 = Z4;
#pragma unroll
      for (int t = 0; t < 4; ++t) { q0 = mfma(xa[2 * t], uv[2 * t], q0); q1 = mfma(xa[2 * t + 1], uv[2 * t + 1], q1); }
      const int M = imax_(imax_(ms.x, ms.y), imax_(ms.z, ms.w));
      f32x4 f;
#pragma unroll
      for (int j = 0; j < 4; ++j) f[j] = asf((127u - umin_((unsigned)(M - ms[j]), 127u)) << 23);
      const float rl = rcp(hsum4(f * ls));
      const float val = opq(hsum4((q0 + q1) * f) * rl);
      const unsigned ov = nosent(pk2(val, dppf<DPP_X1>(val)));
      if (g < 2 && (i16 & 1) == 0) st4(rs, o4, ov);
      STAMP(24);
    } else {
    const bool role7 = (r >> 2) < nl;
    const u16* wg = P.wg + 32 * (role7 ? fg : 0); const u16* wu = P.wu + 32 * (role7 ? fg : 0);
    if (!role7 && w < 2) {
      const int gi = ((xc - 3) * 2 + par) * 128 + tid;
      int off[4];
#pragma unroll
      for (int p = 0; p < 4; ++p) off[p] = WS_MR + p * 20480 + gi * 16;
      nap<DLY_C>();
      v4u mv[4];
      gather<4, 1, 0, 8>(rs, off, mv, tb);
      st16(rs, WS_MC + gi * 16, tag4((untag4(mv[0]) + untag4(mv[1])) + (untag4(mv[2]) + untag4(mv[3])), tb));
      STAMP(22);
    }
    if (role7) {
    const bool role9 = fg < 40;
    const int b9 = role9 ? fg / 20 : 0, p9 = (fg % 20) * 32;
    const int nb9 = (p9 & ~127) + 2 * (p9 & 63) + 4 * ((p9 >> 6) & 1);
    LAS unsigned char* YS = L + L_TILEB;
    LAS float* ROC = (LAS float*)(L + L_TILEB + 16384);
    LAS float* XC = ROC + 1280;
    LAS float* GB = XC + 1280;
    constexpr int T_RED = LT_MISC, T_WST = LT_MISC + 12288, T_HH = T_WST + 512, T_R9 = T_HH + 1024, T_XB = T_R9 + 1024;
    static_assert(T_XB + 8 * 16 * 208 <= L_STAMP - 1024, "tail lds");
    LAS float* red = (LAS float*)(L + T_RED);
    LAS float* wst = (LAS float*)(L + T_WST);
    const int e9 = (tid >> 1) & 31, n9 = nb9 + 8 * (e9 >> 2) + (e9 & 3);
    constexpr int T_RED2 = L_TILEB + 27648;
    const int fl = lane & 31, fb = lane >> 5;
    v4u idf[2];
    float x9 = 0.f;
    v4u wt[8];
    if (w >= 6) {
#pragma unroll 1
      for (int i = 0; i < 5; ++i) { int ii = i + 2 * par; if (ii >= 5) ii -= 5; tile_dma<8, true>(wg, wu, 3456, 320 * (w & 1) + 64 * ii, lane, Lb + L_TILE, 0); }
      WAIT_VM0();
      GSX(21);
    } else {
      const bool mainT = tid < 320;
      const int bb = mainT ? tid / 160 : b9, m4 = mainT ? (tid % 160) * 4 : 0;
      const Row xr0 = row_load(P.x, lane), xr1 = row_load(P.x + 640, lane);
      const v2u xr = *(const GAS v2u*)(P.x + bb * 640 + m4);
      x9 = asf((unsigned)P.x[b9 * 640 + n9] << 16);
      const float mean0 = row_mean(xr0, lane), mean1 = row_mean(xr1, lane);
      float piv = bb ? mean1 : mean0;
      f32x4 xcv = {blo(xr.x) - piv, bhi(xr.x) - piv, blo(xr.y) - piv, bhi(xr.y) - piv};
      const int cA = mainT ? 640 + m4 : 1920 + nb9 + (tid & 7) * 8, cB = mainT ? 1280 + m4 : cA;
      int off[2] = {WS_MC + (bb * 2560 + cA) * 4, WS_MC + (bb * 2560 + cB) * 4};
      PIN(xcv); PIN(x9);
      nap<DLY_G>();
      v4u mv[2];
      gather<2, 1, 0, 12>(rs, off, mv, tb);
      GST(6);
      const f32x4 sb = untag4(mv[0]), cb = untag4(mv[1]);
      if (mainT) {
        const f32x4 oc = {guard_oc(1.f + cb.x), guard_oc(1.f + cb.y), guard_oc(1.f + cb.z), guard_oc(1.f + cb.w)};
        *(LAS f32x4*)(ROC + bb * 640 + m4) = f32x4{rcp(oc.x), rcp(oc.y), rcp(oc.z), rcp(oc.w)};
        *(LAS f32x4*)(XC + bb * 640 + m4) = xcv;
#pragma unroll
        for (int ty = 0; ty < 3; ++ty) {
          const f32x4 y = ty == 0 ? oc : ty == 1 ? sb : xcv * oc;
          const unsigned h0 = pk2(y.x, y.y), h1 = pk2(y.z, y.w);
          *(LAS v2u*)(YS + (ty * 4 + bb * 2) * 1296 + m4 * 2) = v2u{h0, h1};
          *(LAS v2u*)(YS + (ty * 4 + bb * 2 + 1) * 1296 + m4 * 2) = v2u{pk2(y.x - blo(h0), y.y - bhi(h0)), pk2(y.z - blo(h1), y.w - bhi(h1))};
        }
      } else if (tid < 328) {
        *(LAS f32x4*)(GB + (tid & 7) * 4) = sb;
      }
      GST(8);
    }
#pragma unroll
    for (int u = 0; u < 2; ++u) {
      const bool on = (g >> 1) == u && (g & 1) == (i16 >> 3);
      idf[u] = diag_mk(on ? 0x3F80u : 0u, diag_sel(lane, 0));
    }
    const int o5 = WS_H5 + (k0w >> 5) * 1024 + lane * 16;
    int off[3] = {o5, o5 + 1024, w < 4 ? o5 + 2048 : o5};
    unsigned xbw = T_XB + w * 3328 + (((lane >> 2) & 1) * 8 + (lane >> 3)) * 208 + (lane & 3) * 16;
    unsigned xbr = T_XB + w * 3328 + i16 * 208 + g * 16;
    unsigned rdw = T_RED2 + (((w >> 2) * 128 + (g >> 1) * 64 + (g & 1) * 16 + i16) * 4 + (w & 3)) * 4;
    unsigned rdr = T_RED2 + (fb * 64 + fl) * 16;
    unsigned stw = T_WST + (((g >> 1) * 2 + (i16 & 1)) * 16 + w * 2 + (g & 1)) * 4;
    unsigned hhw = T_HH + w * 128 + lane * 2, hhr = T_HH + w * 128 + (i16 >> 3) * 64 + g * 16;
    int o6 = WS_H6 + ((fg * 2 + (g >> 1)) * 640 + 128 * w + 64 * (g & 1) + 4 * i16) * 4;
    PIN(off[0]); PIN(off[2]); PIN(idf[0]); PIN(idf[1]); PIN(xbw); PIN(xbr); PIN(rdw); PIN(rdr); PIN(stw); PIN(hhw); PIN(hhr); PIN(o6);
    BAR();
    GST(13);
    v4u wd[8];
    if (w < 5) {
#pragma unroll
      for (int j = 0; j < 8; ++j) wd[j] = ldnt16(P.wd + (size_t)(32 * fg + 8 * g + j) * 640 + 128 * w + 8 * i16);
    }
    __builtin_amdgcn_sched_barrier(0);
    v4u wf[3][4];
    v4u ya[3];
#pragma unroll
    for (int s = 0; s < 3; ++s) ya[s] = *(LAS v4u*)(YS + (i16 < 12 ? i16 : i16 - 4) * 1296 + (k0w + ((s < 2 || w < 4) ? 32 * s : 0) + 8 * g) * 2);
    if (w < 4) tile_frags<3>(L, L_TILE, k0w, lane, wf);
    else       tile_frags<2>(L, L_TILE, k0w, lane, wf);
    float roc[3], xcs[3];
#pragma unroll
    for (int s = 0; s < 3; ++s) {
      const int k = k0w + ((s < 2 || w < 4) ? 32 * s : 0) + 16 * (g & 1) + i16;
      roc[s] = ROC[(g >> 1) * 640 + k]; xcs[s] = XC[(g >> 1) * 640 + k];
    }
    float gb9 = GB[e9], roc9 = ROC[b9 * 640 + n9];
    {
      f32x4 acc[4];
#pragma unroll
      for (int s = 0; s < 3; ++s) {
        if (s < 2 || w < 4) {
#pragma unroll
          for (int nt = 0; nt < 4; ++nt) acc[nt] = mfma(ya[s], wf[s][nt], s == 0 ? Z4 : acc[nt]);
        }
      }
      if (g < 3) {
#pragma unroll
        for (int nt = 0; nt < 4; ++nt) {
          red[((g * 2 + (w >> 2)) * 128 + 16 * nt + i16) * 4 + (w & 3)] = acc[nt].x + acc[nt].y;
          red[((g * 2 + (w >> 2)) * 128 + 64 + 16 * nt + i16) * 4 + (w & 3)] = acc[nt].z + acc[nt].w;
        }
      }
    }
    PIN(gb9); PIN(roc9);
#pragma unroll
    for (int s = 0; s < 3; ++s) { PIN(roc[s]); PIN(xcs[s]); }
    BAR();
    float cons[3][2];
    if (w < 5) {
#pragma unroll
      for (int ty = 0; ty < 3; ++ty)
#pragma unroll
        for (int gu = 0; gu < 2; ++gu)
          cons[ty][gu] = hsum4(*(LAS f32x4*)(red + ((ty * 2 + 0) * 128 + fb * 64 + gu * 32 + fl) * 4) + *(LAS f32x4*)(red + ((ty * 2 + 1) * 128 + fb * 64 + gu * 32 + fl) * 4));
    }
#pragma unroll
    for (int s = 0; s < 3; ++s) { PIN(cons[s][0]); PIN(cons[s][1]); }
      if (w < 5) {
#pragma unroll
        for (int t = 0; t < 8; ++t)
#pragma unroll
          for (int d = 0; d < 4; ++d) wt[t][d] = __builtin_amdgcn_perm(wd[2 * d + 1][t >> 1], wd[2 * d][t >> 1], (t & 1) ? 0x07060302u : 0x05040100u);
      }
#pragma unroll
      for (int t = 0; t < 8; ++t) PIN(wt[t]);
    GST(14);
    wait_tag64(rs, WS_HINT5 + ((((k0w >> 5) + ((lane & 8) ? (w < 4 ? 2 : 1) : 0)) >> 1) * 8 + (lane & 7)) * 128, tlo, thi);
    GST(15);
    nap<DLY_5>();
    {
      v4u v[3];
      gather<3, 0>(rs, off, v, 0u);
      GST(16);
#pragma unroll
      for (int s = 0; s < 3; ++s) *(LAS v4u*)(L + xbw + 64 * s) = v[s];
      WAIT_LGKM0();
      f32x4 acc[4];
      float s1 = 0.f, s2 = 0.f;
      v4u xa[3];
#pragma unroll
      for (int s = 0; s < 3; ++s) xa[s] = *(LAS v4u*)(L + xbr + 64 * s);
      __builtin_amdgcn_sched_barrier(0);
#pragma unroll
      for (int s = 0; s < 3; ++s) {
        if (s < 2 || w < 4) {
          const v4u a = xa[s];
          const f32x4 i0 = mfma(a, idf[0], Z4), i1 = mfma(a, idf[1], Z4);
#pragma unroll
          for (int nt = 0; nt < 4; ++nt) acc[nt] = mfma(a, wf[s][nt], s == 0 ? Z4 : acc[nt]);
          const float d = merge16(hsum4(i0), hsum4(i1)) * roc[s] + xcs[s];
          s1 += d; s2 += d * d;
        }
      }
      const bool odd = i16 & 1;
      float t = (odd ? s2 : s1) + dppf<DPP_X1>(odd ? s1 : s2);
      t = dadd<DPP_ROR8>(dadd<0x124>(dadd<DPP_X2>(opq(t))));
      if (i16 < 2) *(LAS float*)(L + stw) = t;
      *(LAS float*)(L + rdw) = merge16(hsum4(acc[0]), hsum4(acc[1]));
      *(LAS float*)(L + rdw + 512) = merge16(hsum4(acc[2]), hsum4(acc[3]));
    }
    BAR();
    if (tid == 448) st8(rs, WS_HINT6 + fg * 128, v2u{tlo, thi});
    GST(17);
    if (w < 5) {
      const f32x4 g0 = *(LAS f32x4*)(L + rdr), g1 = *(LAS f32x4*)(L + rdr + 2048), u0 = *(LAS f32x4*)(L + rdr + 512), u1 = *(LAS f32x4*)(L + rdr + 2560);
      const float stv = wst[lane];
      __builtin_amdgcn_sched_barrier(0);
      const float st = sum16l(stv) * (1.f / 640.f);
      auto sw = __builtin_amdgcn_permlane16_swap(asu(st), asu(st), false, false);
      const float mu = asf(sw[0]), e2 = asf(sw[1]);
      const float rsd = __builtin_amdgcn_rsqf(fmaxf(e2 - mu * mu, 0.f) + 1e-5f);
      const float gg = rsd * (hsum4(g0 + g1) + cons[2][0] - mu * cons[0][0]) + cons[1][0];
      const float uu = rsd * (hsum4(u0 + u1) + cons[2][1] - mu * cons[0][1]) + cons[1][1];
      *(LAS u16*)(L + hhw) = (u16)(pk2(uu * sigm(gg), 0.f) & 0xFFFFu);
      WAIT_LGKM0();
      const v4u a = *(LAS v4u*)(L + hhr);
      f32x4 d[8];
#pragma unroll
      for (int t = 0; t < 8; ++t) d[t] = mfma(a, wt[t], Z4);
      const bool hi = g & 1;
      const f32x4 o = {hi ? d[4].x : d[0].x, hi ? d[5].x : d[1].x, hi ? d[6].x : d[2].x, hi ? d[7].x : d[3].x};
      st16(rs, o6, tag4(o, tb));
    }
    GST(18);
    if (role9) {
      const int pp = 7 * w + (lane >> 3);
      const bool act = (lane >> 3) < 7 && pp < 54;
      int off9[1] = {WS_H6 + (((act ? pp : 0) * 2 + b9) * 640 + p9 + (lane & 7) * 4) * 4};
      unsigned am = act ? 0xFFFFFF00u : 0u;
      unsigned r9w = T_R9 + (((w >> 2) * 32 + (lane & 7) * 4 + (g & 1) * 2 + (g >> 1)) * 4 + (w & 3)) * 4;
      unsigned r9r = T_R9 + ((tid & 1) * 32 + e9) * 16;
      float x1 = 0.f;
      if (w == 0) {
        float z = 0.f;
#pragma unroll
        for (int p = 0; p < 4; ++p) {
          const unsigned hv = __builtin_amdgcn_raw_buffer_load_b16(rs, WS_H5 + ((((n9 >> 5) * 8 + (lane & 1) * 4 + p) * 2 + b9) * 32 + (n9 & 31)) * 2, 0, 16);
          z += asf(hv << 16);
        }
        x1 = x9 + dadd<DPP_X1>(z) * roc9;
      }
      PIN(off9[0]); PIN(am); PIN(r9w); PIN(r9r); PIN(x1);
      wait_tag64(rs, WS_HINT6 + (act ? pp : 0) * 128, tlo, thi);
      nap<DLY_6>();
      v4u v[1];
      gather<1, 1>(rs, off9, v, tb);
      GST(19);
      const float a01 = merge32(asf(v[0].x & am), asf(v[0].y & am)), a23 = merge32(asf(v[0].z & am), asf(v[0].w & am));
      const float t = dadd<DPP_ROR8>(merge16(a01, a23));
      if ((lane & 8) == 0) *(LAS float*)(L + r9w) = t;
      BAR();
      if (w == 0) {
        const float sm = dadd<DPP_X1>(hsum4(*(LAS f32x4*)(L + r9r)));
        const float o = opq(x1 + gb9 * sm);
        const unsigned ov = pk2(o, dppf<DPP_X2>(o));
        if ((lane & 3) == 0) *(GAS unsigned*)(P.out + b9 * 640 + n9) = ov;
        if (fg == 0 && tid == 0) st8(rs, WS_EPOCH, v2u{tlo, thi});
      }
      GST(20);
    }
  }
  }
  } else {
    const int ja = r - 16, hg = par, tile = (ja >> 1) * 8 + xc, b = tile >> 5, s = tile & 31;
    const int aidx = tile * 2 + hg;
    const bool roleO = ja < 10;
    const int kr = xc, ng = ja;
    const int wq4 = w & 3;
    v4u cf[32];
    unsigned mcr = LA_MISC + 8192 + i16 * 32;
    if (w < 4) {
      if (!isS1) { pace<DEP_A1>(); nap<DLY_A0>(); }
      const int rA = 32 * wq4 + 16 * hg, rB = 32 * wq4 + 16 - 16 * hg;
      const char* ck = (const char*)P.ckv + (size_t)(tile * 128) * 1024;
#pragma unroll 1
      for (int i = 0; i < 32; ++i) {
        const int row = (i < 16 ? rA : rB - 16) + i;
        dma16(ck + row * 1024, (unsigned)((lane ^ (((i & 3) << 2) | ((i >> 2) & 3))) * 16), Lb + L_TILE + row * 1024);
      }
      const unsigned sw = ((i16 & 3) << 2) | ((i16 >> 2) & 3);
      unsigned qr = LA_Q + i16 * 1040 + g * 16;
      unsigned pwA = LA_P + i16 * 272 + (rA + 4 * g) * 2, pwB = LA_P + i16 * 272 + (rB + 4 * g) * 2;
      unsigned mcw = LA_MISC + 8192 + i16 * 32 + wq4 * 4;
      int o3s = WS_H3S + (((b * 2 + hg) * 32 + s) * 16 + i16) * 16;
      PIN(qr); PIN(pwA); PIN(pwB); PIN(mcw); PIN(o3s); PIN(mcr);
      BAR();
      asm volatile("s_waitcnt vmcnt(16)" ::: "memory");
#pragma unroll
      for (int t = 0; t < 16; ++t) cf[t] = *(LAS v4u*)(L + L_TILE + (rA + i16) * 1024 + (t >> 2) * 256 + (((4 * (t & 3) + g) ^ sw) * 16));
      WAIT_VM0();
      STAMP(2);
#pragma unroll
      for (int t = 0; t < 16; ++t) cf[16 + t] = *(LAS v4u*)(L + L_TILE + (rB + i16) * 1024 + (t >> 2) * 256 + (((4 * (t & 3) + g) ^ sw) * 16));
      {
        v2u e2_ = ep; PIN(e2_);
        const unsigned elo = __builtin_amdgcn_readfirstlane(e2_.x), ehi = __builtin_amdgcn_readfirstlane(e2_.y);
        tlo = elo + 1u; thi = ehi + (tlo == 0u ? 1u : 0u); tb = tlo & 0xFFu;
      }
      WAIT_LGKM0();
      STAMP(3);
      BAR();
      STAMP(6);
      f32x4 s0 = Z4, s1 = Z4;
      v4u qf[16];
#pragma unroll
      for (int t = 0; t < 16; ++t) qf[t] = *(LAS v4u*)(L + qr + t * 64);
      __builtin_amdgcn_sched_barrier(0);
#pragma unroll
      for (int t = 0; t < 16; ++t) { s0 = mfma(cf[t], qf[t], s0); s1 = mfma(cf[16 + t], qf[t], s1); }
      const float mx = fmaxf(fmaxf(fmaxf(s0.x, s0.y), fmaxf(s0.z, s0.w)), fmaxf(fmaxf(s1.x, s1.y), fmaxf(s1.z, s1.w)));
      const float mc = __builtin_ceilf(maxx32(maxx16(mx)));
      float p[8];
#pragma unroll
      for (int j = 0; j < 4; ++j) { p[j] = ex2(s0[j] - mc); p[4 + j] = ex2(s1[j] - mc); }
      *(LAS v2u*)(L + pwA) = v2u{pk2(p[0], p[1]), pk2(p[2], p[3])};
      *(LAS v2u*)(L + pwB) = v2u{pk2(p[4], p[5]), pk2(p[6], p[7])};
      if (g == 0) *(LAS int*)(L + mcw) = (int)mc;
      STAMP(14);
      BAR();
      if (tid == 192) st8(rs, WS_HINT3 + 64 + aidx * 128, v2u{tlo, thi});
      STAMP(8);
      const float ls = addx32(addx16(((p[0] + p[1]) + (p[2] + p[3])) + ((p[4] + p[5]) + (p[6] + p[7]))));
      const v4i mo = *(LAS v4i*)(L + mcr);
      const int M = imax_(imax_(mo.x, mo.y), imax_(mo.z, mo.w));
      if (g == 0) *(LAS float*)(L + mcw + 16) = __builtin_ldexpf(ls, -(int)umin_((unsigned)(M - (int)mc), 255u));
      {
        unsigned old = 0;
        WAIT_LGKM0();
        if (lane == 0) old = __hip_atomic_fetch_add((LAS unsigned*)(L + LA_MISC + 8704), 1u, __ATOMIC_RELAXED, __HIP_MEMORY_SCOPE_WORKGROUP);
        if (__builtin_amdgcn_readfirstlane(old) == 3u) {
          CFENCE();
          const f32x4 lo = *(LAS f32x4*)(L + mcr + 16);
          if (lane < 16) st16(rs, o3s, v4u{(unsigned)M, tlo, asu((lo.x + lo.y) + (lo.z + lo.w)), tlo});
        }
      }
    } else {
      const int hi = r < 26 ? (r - 16) >> 1 : r - 21, mcp = 2 * hi + (xc & 1);
      const bool isM = !isS1 && hi < 10;
      if (isM) {
        const int j0 = 160 * jq + 40 * wq4 + g, jc = j0 + 4 * (i16 < 9 ? i16 : 9);
        const unsigned c0 = *(const GAS u16*)(P.c + jc), c1 = *(const GAS u16*)(P.c + 640 + jc);
        v4u wm[10];
#pragma unroll
        for (int t = 0; t < 10; ++t) wm[t] = ldnt16(P.wmod + (size_t)(j0 + 4 * t) * 3840 + 1280 + 128 * mcp + 8 * i16);
        __builtin_amdgcn_sched_barrier(0);
        const v4u sl = diag_sel(lane, i16 >> 3);
        const unsigned sp = silu_pair(c0, c1);
        f32x4 d = mfma(diag_mk(dppu<DPP_BC + 0>(sp), sl), wm[0], Z4);
        d = mfma(diag_mk(dppu<DPP_BC + 1>(sp), sl), wm[1], d); d = mfma(diag_mk(dppu<DPP_BC + 2>(sp), sl), wm[2], d);
        d = mfma(diag_mk(dppu<DPP_BC + 3>(sp), sl), wm[3], d); d = mfma(diag_mk(dppu<DPP_BC + 4>(sp), sl), wm[4], d);
        d = mfma(diag_mk(dppu<DPP_BC + 5>(sp), sl), wm[5], d); d = mfma(diag_mk(dppu<DPP_BC + 6>(sp), sl), wm[6], d);
        d = mfma(diag_mk(dppu<DPP_BC + 7>(sp), sl), wm[7], d); d = mfma(diag_mk(dppu<DPP_BC + 8>(sp), sl), wm[8], d);
        d = mfma(diag_mk(dppu<DPP_BC + 9>(sp), sl), wm[9], d);
        *(LAS f32x4*)(L + LA_MISC + (wq4 * 64 + lane) * 16) = d;
      }
      {
        const unsigned elo = __builtin_amdgcn_readfirstlane(ep.x), ehi = __builtin_amdgcn_readfirstlane(ep.y);
        tlo = elo + 1u; thi = ehi + (tlo == 0u ? 1u : 0u); tb = tlo & 0xFFu;
      }
      BAR();
      if (isM && w == 7) {
        const LAS f32x4* rp = (const LAS f32x4*)(L + LA_MISC) + lane;
        const f32x4 sm = (rp[0] + rp[64]) + (rp[128] + rp[192]);
        st16(rs, WS_MR + ((jq * 2 + (g >> 1)) * 2560 + 128 * mcp + 8 * i16 + 4 * (g & 1)) * 4, tag4(sm, tb));
      }
      PST(16);
      if (tid == 256) *(LAS unsigned*)(L + LA_MISC + 8704) = 0u;
      const int o = WS_H2 + (((b * 32 + 16 * hg + 4 * wq4 + (g >> 1)) * 2 + (g & 1)) * 512 + 8 * i16) * 2;
      int off[8] = {o, o + 256, o + 512, o + 768, o + 4096, o + 4352, o + 4608, o + 4864};
      v4u sel = diag_mk(((g >> 1) == (i16 >> 3)) ? 0x3F80u : 0u, diag_sel(lane, 0));
      unsigned qw = LA_Q + (4 * wq4 + (g >> 1)) * 1040 + (8 * i16 + 4 * (g & 1)) * 2;
      unsigned prd = LA_P + i16 * 272 + g * 16;
      int o3[2]; o3[0] = WS_H3 + ((16 * hg + 4 * g) * 4 + wq4) * 16384 + (b * 32 + s) * 256 + i16 * 8; o3[1] = o3[0] + 131072;
      unsigned ta0, ta1;
      {
        const unsigned q = i16 >> 2, p = lane & 3;
        const unsigned sw0 = (q << 2) | ((2 * g) & 3), sw1 = sw0 | 1u;
        ta0 = L_TILE + (8 * g + q) * 1024 + wq4 * 256 + (((p >> 1) ^ sw0) * 16) + 8 * (p & 1);
        ta1 = L_TILE + (8 * g + q + 4) * 1024 + wq4 * 256 + (((p >> 1) ^ sw1) * 16) + 8 * (p & 1);
      }
      PIN(ta0); PIN(ta1);
      PIN(off[0]); PIN(off[4]); PIN(sel); PIN(qw); PIN(prd); PIN(o3[0]); PIN(o3[1]); PIN(mcr);
      nap<DLY_T>();
      wait_tag64(rs, lane < 40 ? WS_TOK + lane * 128 : WS_HINT2 + (hg * 64 + 16 * wq4 + (lane & 15)) * 128, tlo, thi);
      PST(4);
      nap<DLY_2>();
      {
        v4u v[8];
        gather<8, 0>(rs, off, v, 0u);
        PST(5);
#pragma unroll
        for (int cg = 0; cg < 8; ++cg) {
          const f32x4 d = mfma(sel, v[cg], Z4);
          *(LAS v2u*)(L + qw + (cg >> 2) * 2080 + (cg & 3) * 256) = v2u{pk2(d.x, d.y), pk2(d.z, d.w)};
        }
      }
      BAR();
      if (tid == 448) st8(rs, WS_HINT3 + aidx * 128, v2u{tlo, thi});
#pragma unroll
      for (int tt = 0; tt < PV_PRE; ++tt)
#pragma unroll
        for (int kt = 0; kt < 4; ++kt) cf[tt * 4 + kt] = trfrag(L, (ta0 ^ (tt * 32)) + kt * 32768, (ta1 ^ (tt * 32)) + kt * 32768);
      PST(15);
      BAR();
      const v4i mo = *(LAS v4i*)(L + mcr);
      v4u pfs[4];
#pragma unroll
      for (int kt = 0; kt < 4; ++kt) pfs[kt] = *(LAS v4u*)(L + prd + kt * 64);
#pragma unroll
      for (int tt = PV_PRE; tt < 8; ++tt)
#pragma unroll
        for (int kt = 0; kt < 4; ++kt) cf[tt * 4 + kt] = trfrag(L, (ta0 ^ (tt * 32)) + kt * 32768, (ta1 ^ (tt * 32)) + kt * 32768);
      __builtin_amdgcn_sched_barrier(0);
      const int M = imax_(imax_(mo.x, mo.y), imax_(mo.z, mo.w));
      v4u a[4];
#pragma unroll
      for (int kt = 0; kt < 4; ++kt) {
        const v4u pf = pfs[kt];
        const unsigned dl = umin_((unsigned)(M - mo[kt]), 255u);
        const u16x2 dd = __builtin_bit_cast(u16x2, dl * 0x00800080u);
#pragma unroll
        for (int j = 0; j < 4; ++j) { const unsigned e = pf[j]; a[kt][j] = __builtin_bit_cast(unsigned, __builtin_elementwise_sub_sat(__builtin_bit_cast(u16x2, e), dd)); }
      }
#pragma unroll
      for (int hf = 0; hf < 2; ++hf) {
        f32x4 d[4];
#pragma unroll
        for (int kt = 0; kt < 4; ++kt)
#pragma unroll
          for (int tt = 0; tt < 4; ++tt) d[tt] = mfma(a[kt], cf[(4 * hf + tt) * 4 + kt], kt == 0 ? Z4 : d[tt]);
#pragma unroll
        for (int j = 0; j < 4; ++j) st8(rs, o3[j >> 1] + (j & 1) * 65536 + hf * 128, v2u{nosent(pk2(d[0][j], d[1][j])), nosent(pk2(d[2][j], d[3][j]))});
      }
      PST(7);
    }
    BAR();
    if (roleO) {
    {
      const u16* wo = P.wo + (size_t)(512 * kr) * 640 + 64 * ng;
      tile_dma<8>(wo, wo + 32, 1280, 64 * w, lane, Lb + L_TILE, 0);
    }
    LAS float* MS = (LAS float*)(L + L_TILEB + 28672);
    if (w == 0) {
      int off[1] = {WS_MC + (((lane >> 4) & 1) * 2560 + (lane >> 5) * 1280 + 64 * ng + i16 * 4) * 4};
      v4u v[1];
      gather<1, 1, 0, 4>(rs, off, v, tb);
      *(LAS f32x4*)(MS + lane * 4) = untag4(v[0]);
    }
    WAIT_VM0();
    BAR();
    OST(13);
    v4u wf[3][4];
    tile_frags<2>(L, L_TILE, 64 * w, lane, wf);
    OST(9);
    const int fo = (tid >> 1) & 127;
    float scl;
    {
      const float ga = MS[fo], cb = MS[128 + fo];
      scl = ga * guard_oc(1.f + cb);
    }
    int off[1] = {WS_H4 + (8 * kr + w) * 512 + (lane & 31) * 16};
    unsigned xbw = LT_MISC + 4096 + w * 1152 + (lane >> 3) * 144 + (lane & 7) * 16;
    unsigned xbr = LT_MISC + 4096 + w * 1152 + (i16 & 3) * 144 + g * 16;
    unsigned rw = LT_MISC + (((w >> 2) * 128 + lane) * 4 + (w & 3)) * 4;
    unsigned rr = LT_MISC + ((tid & 1) * 128 + fo) * 16;
    int o5 = WS_H5 + ((((2 * ng + ((fo >> 5) & 1)) * 8 + kr) * 2 + (fo >> 6)) * 32 + (fo & 31)) * 2;
    PIN(off[0]); PIN(xbw); PIN(xbr); PIN(rw); PIN(rr); PIN(o5); PIN(scl);
    WAIT_LGKM0();
    wait_tag64(rs, WS_HINT4 + (xc * 8 + (w & 6) + (lane & 1)) * 128, tlo, thi);
    OST(10);
    nap<DLY_4>();
    v4u v[1];
    gather<1, 0>(rs, off, v, 0u);
    if (tid == 0) st8(rs, WS_HINT5 + (ng * 8 + kr) * 128, v2u{tlo, thi});
    OST(11);
    *(LAS v4u*)(L + xbw) = v[0];
    WAIT_LGKM0();
    const v4u a0 = *(LAS v4u*)(L + xbr), a1 = *(LAS v4u*)(L + xbr + 64);
    f32x4 acc[4];
#pragma unroll
    for (int nt = 0; nt < 4; ++nt) acc[nt] = mfma(a0, wf[0][nt], Z4);
#pragma unroll
    for (int nt = 0; nt < 4; ++nt) acc[nt] = mfma(a1, wf[1][nt], acc[nt]);
    const f32x4 e = g < 2 ? (g == 0 ? acc[0] : acc[1]) : (g == 2 ? acc[2] : acc[3]);
    *(LAS float*)(L + rw) = e.x + e.y;
    *(LAS float*)(L + rw + 1024) = e.z + e.w;
    BAR();
    if (w < 4) {
      const float z = opq(dadd<DPP_X1>(hsum4(*(LAS f32x4*)(L + rr))) * scl);
      const unsigned ov = nosent(pk2(z, dppf<DPP_X2>(z)));
      if ((lane & 3) == 0) st4(rs, o5, ov);
    }
    OST(12);
    }
  }
  WAIT_VM0();
}

extern "C" void kernel_launch(void* const* d_in, const int* in_sizes, int n_in,
                              void* d_out, int out_size, void* d_ws, size_t ws_size,
                              hipStream_t stream) {
  Params p;
  p.x = (const u16*)d_in[0]; p.ckv = (const u16*)d_in[1]; p.wq = (const u16*)d_in[2]; p.wuk = (const u16*)d_in[3];
  p.wuv = (const u16*)d_in[4]; p.wo = (const u16*)d_in[5]; p.wg = (const u16*)d_in[7]; p.wu = (const u16*)d_in[8];
  p.wd = (const u16*)d_in[9]; p.c = (const u16*)d_in[10]; p.wmod = (const u16*)d_in[11];
  p.out = (u16*)d_out; p.ws = (unsigned char*)d_ws;
  mk_kernel<<<256, 512, 0, stream>>>(p);
}
```

```cpp
#include <hip/hip_runtime.h>
#include <hip/hip_bf16.h>
#include <stdint.h>

typedef unsigned short u16;
typedef unsigned long long u64;
typedef unsigned v4u __attribute__((ext_vector_type(4)));
typedef unsigned v2u __attribute__((ext_vector_type(2)));
typedef int v4i __attribute__((ext_vector_type(4)));
typedef float f32x4 __attribute__((ext_vector_type(4)));
typedef float f32x2 __attribute__((ext_vector_type(2)));
typedef short s16x4 __attribute__((ext_vector_type(4)));
typedef unsigned short u16x2 __attribute__((ext_vector_type(2)));
typedef __bf16 bf16x8_t __attribute__((ext_vector_type(8)));
typedef __bf16 bf16x2_t __attribute__((ext_vector_type(2)));
#define GAS __attribute__((address_space(1)))
#define LAS __attribute__((address_space(3)))
#define DI __device__ __forceinline__

constexpr int WS_EPOCH = 0;
constexpr int WS_HINT2 = 16384;
constexpr int WS_HINT4 = 49152;
constexpr int WS_HINT5 = 65536;
constexpr int WS_H1    = 98304;
constexpr int WS_MR    = WS_H1 + 20480;
constexpr int WS_MC    = WS_MR + 81920;
constexpr int WS_H3S   = WS_MC + 20480;
constexpr int WS_H2    = WS_H3S + 32768;
constexpr int WS_H5    = WS_H2 + 131072;
constexpr int WS_H4    = WS_H5 + 20480;
constexpr int WS_H6    = WS_H4 + 32768;
constexpr int WS_H3    = 1 << 20;
constexpr int WS_BYTES = WS_H3 + (2 << 20);
static_assert(WS_H6 + 276480 <= WS_H3, "ws map");

constexpr int L_TILE = 0;
constexpr int L_TILEB = 81920;
constexpr int LT_MISC = 114688;
constexpr int LA_Q = 131072;
constexpr int LA_P = LA_Q + 16 * 1040;
constexpr int LA_MISC = LA_P + 16 * 272;
constexpr int L_DUMP = 163840 - 1024;
constexpr int LDS_BYTES = 163840;
static_assert(LA_MISC + 9216 <= L_DUMP, "lds map");

constexpr int DLY_3 = 4;
constexpr int DLY_6 = 20;
constexpr int DLY_C = 50, DLY_G = 90;
constexpr int DLY_W = 24;
constexpr int DLY_PV = 3;
constexpr int NHELP = 6;
constexpr int PV_PRE = 5;
constexpr int CODE_WARM = 315;
constexpr int SPIN_CAP = 1 << 18;
constexpr unsigned SENT = 0xFFFFFFFFu;

struct Params {
  const u16 *x, *ckv, *wq, *wuk, *wuv, *wo, *wg, *wu, *wd, *c, *wmod;
  u16* out;
  unsigned char* ws;
};

DI float asf(unsigned u) { return __builtin_bit_cast(float, u); }
DI unsigned asu(float f) { return __builtin_bit_cast(unsigned, f); }
DI unsigned pk2(float a, float b) { f32x2 v = {a, b}; return __builtin_bit_cast(unsigned, __builtin_convertvector(v, bf16x2_t)); }
DI float blo(unsigned w) { return asf(w << 16); }
DI float bhi(unsigned w) { return asf(w & 0xFFFF0000u); }
DI f32x4 mfma(v4u a, v4u b, f32x4 c) { return __builtin_amdgcn_mfma_f32_16x16x32_bf16(__builtin_bit_cast(bf16x8_t, a), __builtin_bit_cast(bf16x8_t, b), c, 0, 0, 0); }
DI float opq(float x) { asm("" : "+v"(x)); return x; }
#define PIN(x) asm volatile("" : "+v"(x))
template <int CTRL> DI unsigned dppu(unsigned v) { return (unsigned)__builtin_amdgcn_update_dpp(0, (int)v, CTRL, 0xf, 0xf, true); }
template <int CTRL> DI float dppf(float v) { return asf(dppu<CTRL>(asu(v))); }
template <int CTRL> DI float dadd(float v) { return opq(v + dppf<CTRL>(v)); }
constexpr int DPP_X1 = 0xB1, DPP_X2 = 0x4E, DPP_HMIR = 0x141, DPP_MIR = 0x140, DPP_ROR8 = 0x128, DPP_BC = 0x150;
DI float sum16l(float v) { return dadd<DPP_MIR>(dadd<DPP_HMIR>(dadd<DPP_X2>(dadd<DPP_X1>(v)))); }
DI float merge16(float a, float b) { auto s = __builtin_amdgcn_permlane16_swap(asu(a), asu(b), false, false); return opq(asf(s[0]) + asf(s[1])); }
DI float merge32(float a, float b) { auto s = __builtin_amdgcn_permlane32_swap(asu(a), asu(b), false, false); return opq(asf(s[0]) + asf(s[1])); }
DI float addx16(float v) { return merge16(v, v); }
DI float addx32(float v) { return merge32(v, v); }
DI float maxx16(float v) { auto s = __builtin_amdgcn_permlane16_swap(asu(v), asu(v), false, false); return fmaxf(asf(s[0]), asf(s[1])); }
DI float maxx32(float v) { auto s = __builtin_amdgcn_permlane32_swap(asu(v), asu(v), false, false); return fmaxf(asf(s[0]), asf(s[1])); }
DI float hsum4(f32x4 a) { const f32x2 t = f32x2{a.x, a.y} + f32x2{a.z, a.w}; return opq(t.x + t.y); }
DI float ex2(float v) { return __builtin_amdgcn_exp2f(v); }
DI float rcp(float v) { return __builtin_amdgcn_rcpf(v); }
DI float sigm(float v) { return rcp(1.f + ex2(-1.4426950408889634f * v)); }
#define WAIT_VM0() asm volatile("s_waitcnt vmcnt(0)" ::: "memory")
#define WAIT_LGKM0() asm volatile("s_waitcnt lgkmcnt(0)" ::: "memory")
#define BAR() asm volatile("s_waitcnt lgkmcnt(0)\n\ts_barrier" ::: "memory")
#define CFENCE() asm volatile("" ::: "memory")
#define LDS_ORDER() CFENCE()
template <int N> DI void nap() {
#pragma unroll
  for (int i = 0; i < N / 64; ++i) __builtin_amdgcn_s_sleep(64);
  if (N % 64) __builtin_amdgcn_s_sleep(N % 64);
}
DI void dma16(const void* base, unsigned voff, unsigned ldsaddr) {
  unsigned keep;
  asm volatile("s_mov_b32 %0, m0\n\ts_mov_b32 m0, %3\n\ts_nop 0\n\tglobal_load_lds_dwordx4 %1, %2\n\ts_mov_b32 m0, %0"
               : "=&s"(keep) : "v"(voff), "s"(base), "s"(ldsaddr) : "memory");
}
DI void dma16v(const void* lane_ptr, unsigned ldsaddr) {
  unsigned keep;
  asm volatile("s_mov_b32 %0, m0\n\ts_mov_b32 m0, %2\n\ts_nop 0\n\tglobal_load_lds_dwordx4 %1, off\n\ts_mov_b32 m0, %0"
               : "=&s"(keep) : "v"(lane_ptr), "s"(ldsaddr) : "memory");
}
typedef __amdgpu_buffer_rsrc_t rsrc_t;
DI v4u ld16(rsrc_t rs, int off) { return __builtin_amdgcn_raw_buffer_load_b128(rs, off, 0, 16); }
DI v2u ld8(rsrc_t rs, int off) { return __builtin_amdgcn_raw_buffer_load_b64(rs, off, 0, 16); }
DI void st16(rsrc_t rs, int off, v4u v) { __builtin_amdgcn_raw_buffer_store_b128(v, rs, off, 0, 16); }
DI void st8(rsrc_t rs, int off, v2u v) { __builtin_amdgcn_raw_buffer_store_b64(v, rs, off, 0, 16); }
DI void st4(rsrc_t rs, int off, unsigned v) { __builtin_amdgcn_raw_buffer_store_b32(v, rs, off, 0, 16); }
DI v4u ldg16(const void* p) { return *(const GAS v4u*)p; }
DI unsigned umin_(unsigned a, unsigned b) { return a < b ? a : b; }
DI unsigned umax_(unsigned a, unsigned b) { return a > b ? a : b; }
DI int imax_(int a, int b) { return a > b ? a : b; }
DI unsigned nosent(unsigned w) { return umin_(w, 0xFFFFFFFEu); }
DI unsigned tagf(float f, unsigned tb) { return (asu(f) & 0xFFFFFF00u) | tb; }
DI v4u tag4(f32x4 f, unsigned tb) { return v4u{tagf(f.x, tb), tagf(f.y, tb), tagf(f.z, tb ^ 0xFFu), tagf(f.w, tb ^ 0xFFu)}; }
DI f32x4 untag4(v4u v) { return f32x4{asf(v.x & 0xFFFFFF00u), asf(v.y & 0xFFFFFF00u), asf(v.z & 0xFFFFFF00u), asf(v.w & 0xFFFFFF00u)}; }
DI unsigned bad0(v4u v) { return umax_(umax_(v.x, v.y), umax_(v.z, v.w)); }
DI unsigned bad1(v4u v, unsigned tb) { return __builtin_amdgcn_bitop3_b32(v.x, v.y, tb, 0x7E) | __builtin_amdgcn_bitop3_b32(v.z, v.w, tb, 0xBD); }
DI unsigned bad3(v4u v, unsigned t) { return __builtin_amdgcn_bitop3_b32(v.x, v.y, t, 0x7E) | __builtin_amdgcn_bitop3_b32(v.z, v.w, t, 0x7E); }
DI unsigned tag16(unsigned t) { return (t & 0xFFu) | ((~t & 0xFFu) << 8); }
DI unsigned bad2(v4u v, unsigned t) { return __builtin_amdgcn_bitop3_b32(v.y, v.w, t, 0xDB); }

template <int N, int MODE0, int NB = 0, int SLP = 0>
DI void gather(rsrc_t rs, const int (&off)[N + NB], v4u (&v)[N + NB], unsigned t0, unsigned t1 = 0) {
#pragma unroll 1
  for (int spins = 0; spins < SPIN_CAP; ++spins) {
    CFENCE();
#pragma unroll
    for (int i = 0; i < N + NB; ++i) v[i] = ld16(rs, off[i]);
    unsigned a = 0, b = 0;
#pragma unroll
    for (int i = 0; i < N; ++i) a = MODE0 == 0 ? umax_(a, bad0(v[i])) : (a | (MODE0 == 3 ? bad3(v[i], t0) : bad1(v[i], t0)));
#pragma unroll
    for (int i = N; i < N + NB; ++i) b |= bad2(v[i], t1);
    const bool ok = (MODE0 == 0 ? a != SENT : (a & (MODE0 == 3 ? 0xFFFFu : 0xFFu)) == 0u) && b == 0u;
    if (__all(ok)) break;
    if (SLP) __builtin_amdgcn_s_sleep(SLP);
  }
  CFENCE();
}
DI void wait_tag64(rsrc_t rs, int off, unsigned tlo, unsigned thi) {
#pragma unroll 1
  for (int spins = 0; spins < SPIN_CAP; ++spins) {
    CFENCE();
    const v2u v = ld8(rs, off);
    if (__all(v.x == tlo && v.y == thi)) break;
  }
  CFENCE();
}
DI v4u trfrag(LAS unsigned char* L, unsigned a0, unsigned a1) {
  const s16x4 x = __builtin_amdgcn_ds_read_tr16_b64_v4i16((LAS s16x4*)(L + a0));
  const s16x4 y = __builtin_amdgcn_ds_read_tr16_b64_v4i16((LAS s16x4*)(L + a1));
  const v2u xx = __builtin_bit_cast(v2u, x), yy = __builtin_bit_cast(v2u, y);
  return v4u{xx.x, xx.y, yy.x, yy.y};
}
DI v4u diag_sel(int lane, unsigned hsel) {
  const unsigned two = ((2 * hsel + 1) << 8) | (2 * hsel);
  const unsigned s = (lane & 1) ? ((two << 16) | 0x0C0Cu) : (0x0C0C0000u | two);
  const int d = (lane & 7) >> 1;
  return v4u{d == 0 ? s : 0x0C0C0C0Cu, d == 1 ? s : 0x0C0C0C0Cu, d == 2 ? s : 0x0C0C0C0Cu, d == 3 ? s : 0x0C0C0C0Cu};
}
DI v4u diag_mk(unsigned v, v4u sel) {
  return v4u{__builtin_amdgcn_perm(0u, v, sel.x), __builtin_amdgcn_perm(0u, v, sel.y), __builtin_amdgcn_perm(0u, v, sel.z), __builtin_amdgcn_perm(0u, v, sel.w)};
}

template <int NI, bool TWO = false>
DI void tile_dma(const void* baseL, const void* baseR, unsigned stride, int k0, int lane, unsigned ldsbase, int rot) {
  const unsigned r8 = lane >> 3, cpos = lane & 7;
  const unsigned chE = (((cpos >> 1) ^ ((r8 >> 1) & 1u)) << 1) | (cpos & 1), chO = chE ^ 4u;
  if (TWO) {
    const char* pE = (const char*)((chE >> 2) ? baseR : baseL) + (r8 * stride + (chE & 3) * 16);
    const char* pO = (const char*)((chO >> 2) ? baseR : baseL) + (r8 * stride + (chO & 3) * 16);
#pragma unroll
    for (int i = 0; i < NI; ++i) {
      int ii = i + rot; if (ii >= NI) ii -= NI;
      dma16v(((i & 1) ? pO : pE) + (size_t)((unsigned)(k0 + 8 * ii) * stride), ldsbase + (unsigned)(k0 + 8 * ii) * 128u);
    }
  } else {
    const unsigned vE = r8 * stride + chE * 16, vO = r8 * stride + chO * 16;
#pragma unroll
    for (int i = 0; i < NI; ++i) {
      int ii = i + rot; if (ii >= NI) ii -= NI;
      dma16((const char*)baseL + (size_t)((unsigned)(k0 + 8 * ii) * stride), (i & 1) ? vO : vE, ldsbase + (unsigned)(k0 + 8 * ii) * 128u);
    }
  }
}
template <int NS>
DI void tile_frags(LAS unsigned char* L, unsigned ldsoff, int k0, int lane, v4u (&w)[3][4]) {
  const unsigned g = lane >> 4, q = (lane & 15) >> 2, p = lane & 3;
  const unsigned f = (q >> 1) | ((g & 1) << 1);
  const unsigned base = ldsoff + (k0 + 8 * g + q) * 128 + 8 * p;
#pragma unroll
  for (int s = 0; s < NS; ++s)
#pragma unroll
    for (int nt = 0; nt < 4; ++nt) {
      const unsigned a = base + s * 4096 + ((nt ^ f) * 32);
      w[s][nt] = trfrag(L, a, a + 512);
    }
}
struct Row { v4u a, b; };
DI Row row_load(const u16* row, int lane) {
  Row r; r.a = *(const GAS v4u*)(row + lane * 8); r.b = *(const GAS v4u*)(row + 512 + (lane & 15) * 8); return r;
}
DI float wave_sum(float v) { return addx32(addx16(sum16l(v))); }
DI float sum8bf(v4u v) { return ((blo(v.x) + bhi(v.x)) + (blo(v.y) + bhi(v.y))) + ((blo(v.z) + bhi(v.z)) + (blo(v.w) + bhi(v.w))); }
DI float row_mean(const Row& r, int lane) { return wave_sum(sum8bf(r.a) + (lane < 16 ? sum8bf(r.b) : 0.f)) * (1.f / 640.f); }
DI float guard_oc(float oc) { return fabsf(oc) < 1e-18f ? 1e-18f : oc; }

DI unsigned silu_pair(unsigned c0, unsigned c1) { const float a = asf(c0 << 16), b = asf(c1 << 16); return pk2(a * sigm(a), b * sigm(b)); }

__global__ void __launch_bounds__(512, 2) mk_kernel(Params P) {
  __shared__ __attribute__((aligned(16))) unsigned char smem[LDS_BYTES];
  LAS unsigned char* L = (LAS unsigned char*)smem;
  const unsigned Lb = (unsigned)(size_t)L;
  const int tid = threadIdx.x, lane = tid & 63;
  const int w = __builtin_amdgcn_readfirstlane(tid >> 6);
  const int bid = blockIdx.x, xc = bid & 7, r = bid >> 3;
  const int g = lane >> 4, i16 = lane & 15;
  const int k0w = umin_(96 * w, 128 + 64 * w);
  const bool isS1 = (unsigned)(r - 16) < 10u && !(r & 1);
  const int cgp = ((r - 16) & 14) + (xc & 1), jq = xc >> 1;
  const int mfb = w >> 2, mfm = 16 * (w & 3) + (lane >> 2), mty = (lane >> 1) & 1;
  unsigned xs0, xs1, xs2, xs3, xs4, xm, c0s, c1s;
  v4u wm0, wm1, wm2, wm3, wm4;
  v2u ep, ep1;
  if (isS1) {
    const unsigned vw = ((8 * jq + w) * 20 + g) * 7680u + (i16 >> 3) * 1280u + (i16 & 7) * 16u + 128u * cgp;
    const unsigned vx = mfb * 1280 + lane * 4, vm = (mfb * 640 + 64 * cgp + mfm) * 2, vc = (160 * jq + 20 * w + g + 4 * (i16 < 4 ? i16 : 4)) * 2;
    asm volatile("global_load_dwordx2 %0, %1, %2 sc1" : "=&v"(ep1) : "v"(0u), "s"(P.ws) : "memory");
    asm volatile("global_load_ushort %6, %15, %22\n\tglobal_load_ushort %7, %15, %22 offset:1280\n\t"
                 "global_load_dword %0, %13, %21\n\tglobal_load_dword %1, %13, %21 offset:256\n\tglobal_load_dword %2, %13, %21 offset:512\n\t"
                 "global_load_dword %3, %13, %21 offset:768\n\tglobal_load_dword %4, %13, %21 offset:1024\n\tglobal_load_ushort %5, %14, %21\n\t"
                 "global_load_dwordx4 %8, %16, %23\n\tglobal_load_dwordx4 %9, %17, %23\n\tglobal_load_dwordx4 %10, %18, %23\n\t"
                 "global_load_dwordx4 %11, %19, %23\n\tglobal_load_dwordx4 %12, %20, %23"
                 : "=&v"(xs0), "=&v"(xs1), "=&v"(xs2), "=&v"(xs3), "=&v"(xs4), "=&v"(xm), "=&v"(c0s), "=&v"(c1s),
                   "=&v"(wm0), "=&v"(wm1), "=&v"(wm2), "=&v"(wm3), "=&v"(wm4)
                 : "v"(vx), "v"(vm), "v"(vc), "v"(vw), "v"(vw + 30720u), "v"(vw + 61440u), "v"(vw + 92160u), "v"(vw + 122880u),
                   "s"(P.x), "s"(P.c), "s"(P.wmod) : "memory");
    v4u sel = diag_sel(lane, i16 >> 3);
    unsigned rw = LA_MISC + ((w >> 2) * 256 + lane) * 16 + (w & 3) * 4;
    unsigned rr = LA_MISC + ((lane & 1) * 256 + (mfm & 3) * 64 + (2 * mfb + ((mfm >> 2) & 1)) * 16 + (mfm >> 3) + 8 * mty) * 16;
    int o1 = WS_H1 + ((((2 * cgp + (mfm >> 5)) * 2 + mfb) * 4 + jq) * 32 + (mfm & 31)) * 4;
    PIN(sel); PIN(rw); PIN(rr); PIN(o1);
    asm volatile("s_waitcnt vmcnt(5)" : "+v"(ep1), "+v"(xs0), "+v"(xs1), "+v"(xs2), "+v"(xs3), "+v"(xs4), "+v"(xm), "+v"(c0s), "+v"(c1s) :: "memory");
    unsigned sp = silu_pair(c0s, c1s);
    const f32x2 e0 = {blo(xs0), bhi(xs0)}, e1 = {blo(xs1), bhi(xs1)}, e2 = {blo(xs2), bhi(xs2)}, e3 = {blo(xs3), bhi(xs3)}, e4 = {blo(xs4), bhi(xs4)};
    const f32x2 es = ((e0 + e1) + (e2 + e3)) + e4;
    const float mu = wave_sum(es.x + es.y) * (1.f / 640.f);
    const f32x2 d0 = e0 - mu, d1 = e1 - mu, d2 = e2 - mu, d3 = e3 - mu, d4 = e4 - mu;
    const f32x2 q2 = d0 * d0 + d1 * d1 + d2 * d2 + d3 * d3 + d4 * d4;
    const float rsd = __builtin_amdgcn_rsqf(wave_sum(q2.x + q2.y) * (1.f / 640.f) + 1e-5f);
    float xn = (asf(xm << 16) - mu) * rsd;
    v4u df0 = diag_mk(dppu<DPP_BC + 0>(sp), sel), df1 = diag_mk(dppu<DPP_BC + 1>(sp), sel), df2 = diag_mk(dppu<DPP_BC + 2>(sp), sel);
    v4u df3 = diag_mk(dppu<DPP_BC + 3>(sp), sel), df4 = diag_mk(dppu<DPP_BC + 4>(sp), sel);
    PIN(df0); PIN(df1); PIN(df2); PIN(df3); PIN(df4); PIN(xn);
    asm volatile("s_waitcnt vmcnt(0)" : "+v"(wm0), "+v"(wm1), "+v"(wm2), "+v"(wm3), "+v"(wm4) :: "memory");
    f32x4 da = mfma(df0, wm0, f32x4{0.f, 0.f, 0.f, 0.f}), db = mfma(df1, wm1, f32x4{0.f, 0.f, 0.f, 0.f});
    da = mfma(df2, wm2, da); db = mfma(df3, wm3, db); da = mfma(df4, wm4, da);
    const f32x4 d = da + db;
    ep = ep1;
    const unsigned tl = __builtin_amdgcn_readfirstlane(ep1.x) + 1u;
#pragma unroll
    for (int j = 0; j < 4; ++j) *(LAS float*)(L + rw + j * 1024) = d[j];
    BAR();
    const float v = dadd<DPP_X1>(hsum4(*(LAS f32x4*)(L + rr)));
    const float hp = xn * (dppf<DPP_X2>(v) + (jq == 0 ? 1.f : 0.f)) + v;
    if ((lane & 3) == 0) __hip_atomic_store((GAS unsigned*)(P.ws + o1), pk2(0.f, hp) | tag16(tl), __ATOMIC_RELAXED, __HIP_MEMORY_SCOPE_AGENT);
  }
  else {
    const bool isP = r < 16;
    const int hi = r < 26 ? (r - 16) >> 1 : r - 21;
    unsigned lP = k0w * 128;
    unsigned oP = lP * 64 + xc * 1024 + (r >> 1) * 128, oA = (hi * 8 + w) * 8192 + xc * 1024;
    asm("" : "+s"(lP), "+s"(oP), "+s"(oA));
    const unsigned off = isP ? oP : oA;
    unsigned lds = Lb + (isP ? lP : (unsigned)L_DUMP);
    const unsigned lstep = isP ? 1024u : 0u;
    const unsigned step = isP ? 65536u : 88u * 8192u;
    const int cnt = isP ? (w < 4 ? 12 : 8) : (hi < 9 ? NHELP : NHELP - 1);
    const unsigned xmask = isP ? 0x40u : 0u;
    unsigned vP = (lane >> 3) * 8192u + (((lane & 7) ^ (g & 1) * 2) * 16u);
    unsigned vA = lane * 16u;
    asm("" : "+v"(vP), "+v"(vA));
    const unsigned vb = isP ? vP : vA;
    asm volatile("" :: "s"(P.wq), "s"(P.ws), "s"(off), "s"(lds), "s"(step), "s"(cnt), "s"(xmask), "s"(lstep), "v"(vb));
    if (r == 31 && w < (CODE_WARM + 63) / 64) {
      const u64 pc = __builtin_amdgcn_s_getpc() & ~127ull;
      unsigned keep;
      asm volatile("s_mov_b32 %0, m0\n\ts_mov_b32 m0, %3\n\ts_nop 0\n\tglobal_load_lds_dword %1, %2\n\ts_mov_b32 m0, %0"
                   : "=&s"(keep) : "v"((tid < CODE_WARM ? tid : 0) * 64), "s"(pc), "s"(lds) : "memory");
    }
    if (!isP && hi < 10) {
      const unsigned sc = (3u - ((unsigned)(size_t)P.wq >> 7)) & 7u;
      dma16((const char*)P.wq + ((hi * 8 + w) * 65536 + xc * 1024 + sc * 128), vP, lds);
      const unsigned s63 = (63u - ((unsigned)(size_t)P.wq >> 7)) & 63u;
      if ((s63 >> 3) == (unsigned)xc) dma16((const char*)P.wq + ((hi * 8 + w) * 65536 + s63 * 128), vP, lds);
    }
    if (!isP) {
      const int row = (w < 4 ? 96 * w + 95 : 191 + 64 * w) - hi;
      dma16((const char*)P.wq + (row * 8192 + xc * 1024), vA, lds);
    }
    const char* base = (const char*)(isP ? P.wq : P.wuk) + off;
#pragma unroll 1
    for (int i = 0; i < cnt; ++i) {
      dma16(base, vb ^ ((i << 6) & xmask), lds);
      base += step; lds += lstep;
    }
    ep = __builtin_bit_cast(v2u, __hip_atomic_load((GAS u64*)(P.ws + WS_EPOCH), __ATOMIC_RELAXED, __HIP_MEMORY_SCOPE_AGENT));
    CFENCE();
  }
  const rsrc_t rs = __builtin_amdgcn_make_buffer_rsrc((void*)P.ws, 0, WS_BYTES, 0x00020000);
  const v4u SENT4 = v4u{SENT, SENT, SENT, SENT};
  const f32x4 Z4 = {0.f, 0.f, 0.f, 0.f};
  const int par = r & 1;
  unsigned tlo, thi, tb;

  if (r < 16) {
    const int h = 4 * xc + (r >> 2), sub = r & 3, dh = sub >> 1, ch = sub & 1, hg = xc >> 2;
    const int pidx = xc * 16 + r;
    {
      const u16* wk = P.wuk + h * 128 + dh * 64 + (size_t)(256 * ch + 32 * w) * 4096;
#pragma unroll
      for (int i = 0; i < 4; ++i) {
        const unsigned lr = 8 * i + (lane >> 3);
        dma16(wk, lr * 8192u + (((lane & 7) ^ ((lr >> 1) & 7u)) * 16), Lb + L_TILEB + w * 4096 + i * 1024);
      }
      const int c3 = WS_H3 + (h * 4 + sub) * 16384 + tid * 16;
      st16(rs, c3, SENT4); st16(rs, c3 + 8192, SENT4);
      const int c2 = WS_H2 + ((((lane >> 5) * 32 + h) * 2 + dh) * 512 + 256 * ch) * 2 + (lane & 31) * 16;
      const int c4 = WS_H4 + ((h * 4 + ((lane >> 3) & 3)) * 2 + ch) * 128 + (lane & 7) * 16;
      st16(rs, w == 0 ? c2 : (w == 1 && sub < 2) ? c4 : WS_BYTES, SENT4);
    }
    const int nl = xc < 3 ? 4 : 3, lc0 = 3 * xc + (xc < 3 ? xc : 3);
    const int fg = 2 * (lc0 + (r >> 2)) + par;
    WAIT_VM0();
    {
      v2u e2_ = ep; PIN(e2_);
      tlo = __builtin_amdgcn_readfirstlane(e2_.x) + 1u; thi = ~tlo; tb = tlo & 0xFFu;
    }
    {
      v4u wf[3][4];
      v4u uk[2][2];
      if (w < 4) tile_frags<3>(L, L_TILE, k0w, lane, wf);
      else       tile_frags<2>(L, L_TILE, k0w, lane, wf);
#pragma unroll
      for (int u = 0; u < 2; ++u)
#pragma unroll
        for (int t = 0; t < 2; ++t) uk[u][t] = *(LAS v4u*)(L + L_TILEB + w * 4096 + (2 * i16 + u) * 128 + (((4 * t + g) ^ (i16 & 7)) * 16));
      const int o = WS_H1 + (k0w >> 5) * 1024 + lane * 16;
      int off[3] = {o, o + 1024, w < 4 ? o + 2048 : o};
      unsigned xbw = LT_MISC + 4096 + w * 1664 + (lane >> 3) * 208 + (lane & 7) * 8;
      unsigned xbr = LT_MISC + 4096 + w * 1664 + (i16 & 7) * 208 + g * 16;
      unsigned qxw = LT_MISC + (8 * (g & 1) + w) * 144 + ((g >> 1) * 32 + i16) * 2;
      unsigned qxr = LT_MISC + i16 * 144 + g * 16;
      int o2 = WS_H2 + ((((g >> 1) * 32 + h) * 2 + dh) * 512 + 256 * ch + 32 * w + 2 * i16) * 2;
      PIN(off[0]); PIN(off[2]); PIN(xbw); PIN(xbr); PIN(qxw); PIN(qxr); PIN(o2);
      WAIT_LGKM0();
      v4u v[3];
      gather<3, 3>(rs, off, v, tag16(tlo));
      WAIT_VM0();
#pragma unroll
      for (int s = 0; s < 3; ++s) *(LAS v2u*)(L + xbw + 64 * s) = v2u{__builtin_amdgcn_perm(v[s].y, v[s].x, 0x07060302u), __builtin_amdgcn_perm(v[s].w, v[s].z, 0x07060302u)};
      LDS_ORDER();
      f32x4 acc[4];
      v4u xa[3];
#pragma unroll
      for (int s = 0; s < 3; ++s) xa[s] = *(LAS v4u*)(L + xbr + 64 * s);
      __builtin_amdgcn_sched_barrier(0);
#pragma unroll
      for (int s = 0; s < 3; ++s) {
        if (s < 2 || w < 4) {
#pragma unroll
          for (int nt = 0; nt < 4; ++nt) acc[nt] = mfma(xa[s], wf[s][nt], s == 0 ? Z4 : acc[nt]);
        }
      }
      const float sc = 0.08838834764831845f * 1.4426950408889634f;
      const float h0 = hsum4(acc[0]), h1 = hsum4(acc[1]), h2 = hsum4(acc[2]), h3 = hsum4(acc[3]);
      const unsigned pe = pk2((g < 2 ? h0 : h2) * sc, (g < 2 ? h1 : h3) * sc);
      *(LAS u16*)(L + qxw) = (u16)(pe & 0xFFFFu); *(LAS u16*)(L + qxw + 32) = (u16)(pe >> 16);
      BAR();
      if (tid == 448) st8(rs, WS_HINT2 + pidx * 128, v2u{tlo, thi});
      const v4u qa0 = *(LAS v4u*)(L + qxr), qa1 = *(LAS v4u*)(L + qxr + 64);
      f32x4 d0 = mfma(qa0, uk[0][0], Z4), d1 = mfma(qa0, uk[1][0], Z4);
      d0 = mfma(qa1, uk[0][1], d0); d1 = mfma(qa1, uk[1][1], d1);
      const unsigned ov = nosent(pk2(addx16(hsum4(d0)), addx16(hsum4(d1))));
      if ((g & 1) == 0) st4(rs, o2, ov);
    }
    nap<DLY_W>();
    if (sub < 2) {
      {
        const u16* wv = P.wuv + (size_t)(256 * sub) * 4096 + h * 128;
        tile_dma<4>(wv, wv + 32, 8192, 32 * w, lane, Lb + L_TILE, 0);
        tile_dma<4>(wv + 64, wv + 96, 8192, 32 * w, lane, Lb + L_TILE + 32768, 0);
      }
      WAIT_VM0();
      BAR();
      v4u uv[8];
      {
        const unsigned q = i16 >> 2, p = lane & 3;
#pragma unroll
        for (int t = 0; t < 8; ++t) {
          const unsigned f = (g & 1) | ((t & 1) << 1);
          const unsigned a = L_TILE + (w >> 2) * 32768 + (64 * (t >> 1) + 16 * q + 8 * (t & 1) + 2 * g) * 128 + 8 * p + (((w & 3) ^ f) * 32);
          uv[t] = trfrag(L, a, a + 128);
        }
      }
      const int b = w >> 2, sq = w & 3;
      const int od = WS_H3 + (h * 4 + 2 * sub) * 16384 + (b * 32 + 8 * sq) * 256 + lane * 16;
      const int os = WS_H3S + (((b * 2 + hg) * 32 + 8 * sq + g) * 16 + (h & 15)) * 16;
      int off[4] = {od, od + 1024, od + 16384, od + 17408};
      int offs[2] = {os, os + 1024};
      v4u sl0 = diag_sel(lane, 0), sl1 = diag_sel(lane, 1);
      unsigned x5w = LT_MISC + 4096 + w * 528 + (g >> 1) * 256 + (8 * i16 + 4 * (g & 1)) * 2;
      unsigned x5r = LT_MISC + 4096 + (i16 & 7) * 528 + g * 16 + (i16 >> 3) * 256;
      unsigned stw = LT_MISC + 8704 + b * 32 + sq * 4, str = LT_MISC + 8704 + (g & 1) * 32;
      int o4 = WS_H4 + ((((h * 2 + (w >> 2)) * 2 + (g & 1)) * 2 + sub) * 64 + 16 * (w & 3) + i16) * 2;
      PIN(off[0]); PIN(off[2]); PIN(offs[0]); PIN(sl0); PIN(sl1); PIN(x5w); PIN(x5r); PIN(stw); PIN(str); PIN(o4);
      WAIT_LGKM0();
      v4u sv[2];
      gather<0, 0, 2>(rs, offs, sv, 0u, tlo);
      nap<DLY_3>();
      v4u v[4];
      CFENCE();
#pragma unroll
      for (int i = 0; i < 4; ++i) v[i] = ld16(rs, off[i]);
      CFENCE();
      int mw = imax_((int)sv[0].x, (int)sv[1].x);
      { auto sw = __builtin_amdgcn_permlane16_swap((unsigned)mw, (unsigned)mw, false, false); mw = imax_((int)sw[0], (int)sw[1]); }
      { auto sw = __builtin_amdgcn_permlane32_swap((unsigned)mw, (unsigned)mw, false, false); mw = imax_((int)sw[0], (int)sw[1]); }
      const unsigned e0 = 127u - umin_((unsigned)(mw - (int)sv[0].x), 127u), e1 = 127u - umin_((unsigned)(mw - (int)sv[1].x), 127u);
      const unsigned pe = (e0 << 7) | (e1 << 23);
      v4u a0 = diag_mk(pe, sl0), a1 = diag_mk(pe, sl1);
      const float lw = addx32(addx16(asf(e0 << 23) * asf(sv[0].z) + asf(e1 << 23) * asf(sv[1].z)));
      *(LAS int*)(L + stw) = mw; *(LAS float*)(L + stw + 16) = lw;
      PIN(a0); PIN(a1);
      if (!__all(umax_(umax_(bad0(v[0]), bad0(v[1])), umax_(bad0(v[2]), bad0(v[3]))) != SENT)) gather<4, 0>(rs, off, v, 0u);
      f32x4 d0 = mfma(a0, v[0], Z4), d1 = mfma(a0, v[2], Z4);
      d0 = mfma(a1, v[1], d0); d1 = mfma(a1, v[3], d1);
      const f32x4 d = g < 2 ? d0 : d1;
      *(LAS v2u*)(L + x5w) = v2u{pk2(d.x, d.y), pk2(d.z, d.w)};
      BAR();
      if (tid == 448) st8(rs, WS_HINT4 + (h * 2 + sub) * 128, v2u{tlo, thi});
      const v4i ms = *(LAS v4i*)(L + str);
      const f32x4 ls = *(LAS f32x4*)(L + str + 16);
      v4u xa[4];
#pragma unroll
      for (int t = 0; t < 4; ++t) xa[t] = *(LAS v4u*)(L + x5r + t * 64);
      __builtin_amdgcn_sched_barrier(0);
      f32x4 q0 = Z4, q1 = Z4;
#pragma unroll
      for (int t = 0; t < 4; ++t) { q0 = mfma(xa[t], uv[t], q0); q1 = mfma(xa[t], uv[4 + t], q1); }
      const int M = imax_(imax_(ms.x, ms.y), imax_(ms.z, ms.w));
      f32x4 f;
#pragma unroll
      for (int j = 0; j < 4; ++j) f[j] = asf((127u - umin_((unsigned)(M - ms[j]), 127u)) << 23);
      const float rl = rcp(hsum4(f * ls));
      const float val = opq(addx32(hsum4((g < 2 ? q0 : q1) * f)) * rl);
      const unsigned ov = nosent(pk2(val, dppf<DPP_X1>(val)));
      if (g < 2 && (i16 & 1) == 0) st4(rs, o4, ov);
    } else {
    const bool role7 = (r >> 2) < nl;
    const u16* wg = P.wg + 32 * (role7 ? fg : 0); const u16* wu = P.wu + 32 * (role7 ? fg : 0);
    if (!role7 && w < 2) {
      const int gi = ((xc - 3) * 2 + par) * 128 + tid;
      int off[4];
#pragma unroll
      for (int p = 0; p < 4; ++p) off[p] = WS_MR + p * 20480 + gi * 16;
      nap<DLY_C>();
      v4u mv[4];
      gather<4, 1, 0, 8>(rs, off, mv, tb);
      st16(rs, WS_MC + gi * 16, tag4((untag4(mv[0]) + untag4(mv[1])) + (untag4(mv[2]) + untag4(mv[3])), tb));
    }
    if (role7) {
    const bool role9 = fg < 40;
    const int b9 = role9 ? fg / 20 : 0, p9 = (fg % 20) * 32;
    const int nb9 = (p9 & ~127) + 2 * (p9 & 63) + 4 * ((p9 >> 6) & 1);
    LAS unsigned char* YS = L + L_TILEB;
    LAS float* ROC = (LAS float*)(L + L_TILEB + 16384);
    LAS float* XC = ROC + 1280;
    LAS float* GB = XC + 1280;
    constexpr int T_RED = LT_MISC, T_WST = LT_MISC + 12288, T_HH = T_WST + 512, T_R9 = T_HH + 1024, T_XB = T_R9 + 1024;
    static_assert(T_XB + 8 * 16 * 208 <= L_DUMP, "tail lds");
    LAS float* red = (LAS float*)(L + T_RED);
    LAS float* wst = (LAS float*)(L + T_WST);
    const int e9 = (tid >> 1) & 31, n9 = nb9 + 8 * (e9 >> 2) + (e9 & 3);
    constexpr int T_RED2 = L_TILEB + 27648;
    const int fl = lane & 31, fb = lane >> 5;
    v4u idf[2];
    float x9 = 0.f;
    v4u wt[8];
    if (w >= 6) {
#pragma unroll 1
      for (int i = 0; i < 5; ++i) { int ii = i + 2 * par; if (ii >= 5) ii -= 5; tile_dma<8, true>(wg, wu, 3456, 320 * (w & 1) + 64 * ii, lane, Lb + L_TILE, 0); }
      WAIT_VM0();
    } else {
      const bool mainT = tid < 320;
      const int bb = mainT ? tid / 160 : b9, m4 = mainT ? (tid % 160) * 4 : 0;
      const Row xr0 = row_load(P.x, lane), xr1 = row_load(P.x + 640, lane);
      const v2u xr = *(const GAS v2u*)(P.x + bb * 640 + m4);
      x9 = asf((unsigned)P.x[b9 * 640 + n9] << 16);
      const float mean0 = row_mean(xr0, lane), mean1 = row_mean(xr1, lane);
      float piv = bb ? mean1 : mean0;
      f32x4 xcv = {blo(xr.x) - piv, bhi(xr.x) - piv, blo(xr.y) - piv, bhi(xr.y) - piv};
      const int cA = mainT ? 640 + m4 : 1920 + nb9 + (tid & 7) * 8, cB = mainT ? 1280 + m4 : cA;
      int off[2] = {WS_MC + (bb * 2560 + cA) * 4, WS_MC + (bb * 2560 + cB) * 4};
      PIN(xcv); PIN(x9);
      nap<DLY_G>();
      v4u mv[2];
      gather<2, 1, 0, 12>(rs, off, mv, tb);
      const f32x4 sb = untag4(mv[0]), cb = untag4(mv[1]);
      if (mainT) {
        const f32x4 oc = {guard_oc(1.f + cb.x), guard_oc(1.f + cb.y), guard_oc(1.f + cb.z), guard_oc(1.f + cb.w)};
        *(LAS f32x4*)(ROC + bb * 640 + m4) = f32x4{rcp(oc.x), rcp(oc.y), rcp(oc.z), rcp(oc.w)};
        *(LAS f32x4*)(XC + bb * 640 + m4) = xcv;
#pragma unroll
        for (int ty = 0; ty < 3; ++ty) {
          const f32x4 y = ty == 0 ? oc : ty == 1 ? sb : xcv * oc;
          const unsigned h0 = pk2(y.x, y.y), h1 = pk2(y.z, y.w);
          *(LAS v2u*)(YS + (ty * 4 + bb * 2) * 1296 + m4 * 2) = v2u{h0, h1};
          *(LAS v2u*)(YS + (ty * 4 + bb * 2 + 1) * 1296 + m4 * 2) = v2u{pk2(y.x - blo(h0), y.y - bhi(h0)), pk2(y.z - blo(h1), y.w - bhi(h1))};
        }
      } else if (tid < 328) {
        *(LAS f32x4*)(GB + (tid & 7) * 4) = sb;
      }
    }
#pragma unroll
    for (int u = 0; u < 2; ++u) {
      const bool on = (g >> 1) == u && (g & 1) == (i16 >> 3);
      idf[u] = diag_mk(on ? 0x3F80u : 0u, diag_sel(lane, 0));
    }
    const int o5 = WS_H5 + (k0w >> 5) * 1024 + lane * 16;
    int off[3] = {o5, o5 + 1024, w < 4 ? o5 + 2048 : o5};
    unsigned xbw = T_XB + w * 3328 + (((lane >> 2) & 1) * 8 + (lane >> 3)) * 208 + (lane & 3) * 16;
    unsigned xbr = T_XB + w * 3328 + i16 * 208 + g * 16;
    unsigned rdw = T_RED2 + (((w >> 2) * 128 + (g >> 1) * 64 + (g & 1) * 16 + i16) * 4 + (w & 3)) * 4;
    unsigned rdr = T_RED2 + (fb * 64 + fl) * 16;
    unsigned stw = T_WST + (((g >> 1) * 2 + (i16 & 1)) * 16 + w * 2 + (g & 1)) * 4;
    unsigned hhw = T_HH + w * 128 + lane * 2, hhr = T_HH + w * 128 + (i16 >> 3) * 64 + g * 16;
    int o6 = WS_H6 + ((fg * 2 + (g >> 1)) * 640 + 128 * w + 64 * (g & 1) + 4 * i16) * 4;
    unsigned x1w = L_TILE + ((g >> 1) * 640 + k0w + 16 * (g & 1) + i16) * 4;
    int off9[3]; unsigned am = (lane >> 3) < 2 ? 0xFFFFFF00u : 0u;
#pragma unroll
    for (int i = 0; i < 3; ++i) { const int pl = 8 * i + (lane >> 3); off9[i] = WS_H6 + (((18 * (w < 5 ? 0 : w - 5) + (pl < 18 ? pl : 0)) * 2 + b9) * 640 + p9 + (lane & 7) * 4) * 4; }
    unsigned r9w = T_R9 + (((lane & 7) * 4 + (g & 1) * 2 + (g >> 1)) * 4 + (w & 3)) * 4;
    PIN(x1w); PIN(off9[0]); PIN(off9[1]); PIN(off9[2]); PIN(am); PIN(r9w);
    PIN(off[0]); PIN(off[2]); PIN(idf[0]); PIN(idf[1]); PIN(xbw); PIN(xbr); PIN(rdw); PIN(rdr); PIN(stw); PIN(hhw); PIN(hhr); PIN(o6);
    BAR();
    v4u wd[8];
    if (w < 5) {
#pragma unroll
      for (int j = 0; j < 8; ++j) wd[j] = ldg16(P.wd + (size_t)(32 * fg + 8 * g + j) * 640 + 128 * w + 8 * i16);
    }
    __builtin_amdgcn_sched_barrier(0);
    v4u wf[3][4];
    v4u ya[3];
#pragma unroll
    for (int s = 0; s < 3; ++s) ya[s] = *(LAS v4u*)(YS + (i16 < 12 ? i16 : i16 - 4) * 1296 + (k0w + ((s < 2 || w < 4) ? 32 * s : 0) + 8 * g) * 2);
    if (w < 4) tile_frags<3>(L, L_TILE, k0w, lane, wf);
    else       tile_frags<2>(L, L_TILE, k0w, lane, wf);
    float roc[3], xcs[3];
#pragma unroll
    for (int s = 0; s < 3; ++s) {
      const int k = k0w + ((s < 2 || w < 4) ? 32 * s : 0) + 16 * (g & 1) + i16;
      roc[s] = ROC[(g >> 1) * 640 + k]; xcs[s] = XC[(g >> 1) * 640 + k];
    }
    float gb9 = GB[e9], piv9 = x9 - XC[b9 * 640 + n9];
    {
      f32x4 acc[4];
#pragma unroll
      for (int s = 0; s < 3; ++s) {
        if (s < 2 || w < 4) {
#pragma unroll
          for (int nt = 0; nt < 4; ++nt) acc[nt] = mfma(ya[s], wf[s][nt], s == 0 ? Z4 : acc[nt]);
        }
      }
      if (g < 3) {
#pragma unroll
        for (int nt = 0; nt < 4; ++nt) {
          red[((g * 2 + (w >> 2)) * 128 + 16 * nt + i16) * 4 + (w & 3)] = acc[nt].x + acc[nt].y;
          red[((g * 2 + (w >> 2)) * 128 + 64 + 16 * nt + i16) * 4 + (w & 3)] = acc[nt].z + acc[nt].w;
        }
      }
    }
    PIN(gb9); PIN(piv9);
#pragma unroll
    for (int s = 0; s < 3; ++s) { PIN(roc[s]); PIN(xcs[s]); }
    BAR();
    float cons[3][2];
    if (w < 5) {
#pragma unroll
      for (int ty = 0; ty < 3; ++ty)
#pragma unroll
        for (int gu = 0; gu < 2; ++gu)
          cons[ty][gu] = hsum4(*(LAS f32x4*)(red + ((ty * 2 + 0) * 128 + fb * 64 + gu * 32 + fl) * 4) + *(LAS f32x4*)(red + ((ty * 2 + 1) * 128 + fb * 64 + gu * 32 + fl) * 4));
    }
#pragma unroll
    for (int s = 0; s < 3; ++s) { PIN(cons[s][0]); PIN(cons[s][1]); }
      if (w < 5) {
#pragma unroll
        for (int t = 0; t < 8; ++t)
#pragma unroll
          for (int d = 0; d < 4; ++d) wt[t][d] = __builtin_amdgcn_perm(wd[2 * d + 1][t >> 1], wd[2 * d][t >> 1], (t & 1) ? 0x07060302u : 0x05040100u);
      }
#pragma unroll
      for (int t = 0; t < 8; ++t) PIN(wt[t]);
    wait_tag64(rs, WS_HINT5 + ((((k0w >> 5) + ((lane & 32) ? (w < 4 ? 2 : 1) : 0)) >> 1) * 8 + ((lane >> 2) & 7)) * 128, tlo, thi);
    {
      v4u v[3];
      gather<3, 0>(rs, off, v, 0u);
#pragma unroll
      for (int s = 0; s < 3; ++s) *(LAS v4u*)(L + xbw + 64 * s) = v[s];
      LDS_ORDER();
      f32x4 acc[4];
      float s1 = 0.f, s2 = 0.f;
      v4u xa[3];
#pragma unroll
      for (int s = 0; s < 3; ++s) xa[s] = *(LAS v4u*)(L + xbr + 64 * s);
      __builtin_amdgcn_sched_barrier(0);
#pragma unroll
      for (int s = 0; s < 3; ++s) {
        if (s < 2 || w < 4) {
          const v4u a = xa[s];
          const f32x4 i0 = mfma(a, idf[0], Z4), i1 = mfma(a, idf[1], Z4);
#pragma unroll
          for (int nt = 0; nt < 4; ++nt) acc[nt] = mfma(a, wf[s][nt], s == 0 ? Z4 : acc[nt]);
          const float d = merge16(hsum4(i0), hsum4(i1)) * roc[s] + xcs[s];
          s1 += d; s2 += d * d;
          *(LAS float*)(L + x1w + 128 * s) = d;
        }
      }
      const bool odd = i16 & 1;
      float t = (odd ? s2 : s1) + dppf<DPP_X1>(odd ? s1 : s2);
      t = dadd<DPP_ROR8>(dadd<0x124>(dadd<DPP_X2>(opq(t))));
      if (i16 < 2) *(LAS float*)(L + stw) = t;
      *(LAS float*)(L + rdw) = merge16(hsum4(acc[0]), hsum4(acc[1]));
      *(LAS float*)(L + rdw + 512) = merge16(hsum4(acc[2]), hsum4(acc[3]));
    }
    BAR();
    if (w < 5) {
      const f32x4 g0 = *(LAS f32x4*)(L + rdr), g1 = *(LAS f32x4*)(L + rdr + 2048), u0 = *(LAS f32x4*)(L + rdr + 512), u1 = *(LAS f32x4*)(L + rdr + 2560);
      const float stv = wst[lane];
      __builtin_amdgcn_sched_barrier(0);
      const float st = sum16l(stv) * (1.f / 640.f);
      auto sw = __builtin_amdgcn_permlane16_swap(asu(st), asu(st), false, false);
      const float mu = asf(sw[0]), e2 = asf(sw[1]);
      const float rsd = __builtin_amdgcn_rsqf(fmaxf(e2 - mu * mu, 0.f) + 1e-5f);
      const float gg = rsd * (hsum4(g0 + g1) + cons[2][0] - mu * cons[0][0]) + cons[1][0];
      const float uu = rsd * (hsum4(u0 + u1) + cons[2][1] - mu * cons[0][1]) + cons[1][1];
      *(LAS u16*)(L + hhw) = (u16)(pk2(uu * sigm(gg), 0.f) & 0xFFFFu);
      LDS_ORDER();
      const v4u a = *(LAS v4u*)(L + hhr);
      f32x4 d[8];
#pragma unroll
      for (int t = 0; t < 8; ++t) d[t] = mfma(a, wt[t], Z4);
      const bool hi = g & 1;
      const f32x4 o = {hi ? d[4].x : d[0].x, hi ? d[5].x : d[1].x, hi ? d[6].x : d[2].x, hi ? d[7].x : d[3].x};
      st16(rs, o6, tag4(o, tb));
    }
    if (role9) {
      float x1 = 0.f;
      if (w >= 5) {
        nap<DLY_6>();
        v4u v[3];
        gather<3, 1>(rs, off9, v, tb);
        const f32x4 t4 = (untag4(v[0]) + untag4(v[1])) + f32x4{asf(v[2].x & am), asf(v[2].y & am), asf(v[2].z & am), asf(v[2].w & am)};
        const float a01 = merge32(t4.x, t4.y), a23 = merge32(t4.z, t4.w);
        const float t = dadd<DPP_ROR8>(merge16(a01, a23));
        if ((lane & 8) == 0) *(LAS float*)(L + r9w) = t;
      } else if (w == 0) {
        x1 = *(LAS float*)(L + L_TILE + (b9 * 640 + n9) * 4) + piv9;
        PIN(x1);
      }
      BAR();
      if (w == 0) {
        const f32x4 q = *(LAS f32x4*)(L + T_R9 + e9 * 16);
        const float o = opq(x1 + gb9 * ((q.y + q.z) + q.w));
        const unsigned ov = pk2(o, dppf<DPP_X2>(o));
        if ((lane & 3) == 0) *(GAS unsigned*)(P.out + b9 * 640 + n9) = ov;
        if (fg == 0 && tid == 0) st8(rs, WS_EPOCH, v2u{tlo, thi});
      }
    }
  }
  }
  } else {
    const int ja = r - 16, hg = par, tile = (ja >> 1) * 8 + xc, b = tile >> 5, s = tile & 31;
    const bool roleO = ja < 10;
    const int kr = xc, ng = ja;
    const int wq4 = w & 3;
    v4u cf[32];
    unsigned mcr = LA_MISC + 8192 + i16 * 32;
    if (w < 4) {
      if (!isS1) WAIT_VM0();
      const int rA = 32 * wq4 + 16 * hg, rB = 32 * wq4 + 16 - 16 * hg;
      const char* ck = (const char*)P.ckv + (size_t)(tile * 128) * 1024;
#pragma unroll 1
      for (int i = 0; i < 32; ++i) {
        const int row = (i < 16 ? rA : rB - 16) + i;
        dma16(ck + row * 1024, (unsigned)((lane ^ (((i & 3) << 2) | ((i >> 2) & 3))) * 16), Lb + L_TILE + row * 1024);
      }
      const unsigned sw = ((i16 & 3) << 2) | ((i16 >> 2) & 3);
      unsigned qr = LA_Q + i16 * 1040 + g * 16;
      unsigned pwA = LA_P + i16 * 272 + (rA + 4 * g) * 2, pwB = LA_P + i16 * 272 + (rB + 4 * g) * 2;
      unsigned mcw = LA_MISC + 8192 + i16 * 32 + wq4 * 4;
      int o3s = WS_H3S + (((b * 2 + hg) * 32 + s) * 16 + i16) * 16;
      PIN(qr); PIN(pwA); PIN(pwB); PIN(mcw); PIN(o3s); PIN(mcr);
      BAR();
      asm volatile("s_waitcnt vmcnt(16)" ::: "memory");
#pragma unroll
      for (int t = 0; t < 16; ++t) cf[t] = *(LAS v4u*)(L + L_TILE + (rA + i16) * 1024 + (t >> 2) * 256 + (((4 * (t & 3) + g) ^ sw) * 16));
      WAIT_VM0();
      {
        v2u e2_ = ep; PIN(e2_);
        tlo = __builtin_amdgcn_readfirstlane(e2_.x) + 1u; thi = ~tlo; tb = tlo & 0xFFu;
      }
      if (roleO && tid < 16) st16(rs, WS_H5 + ((2 * ng + (tid >> 3)) * 8 + kr) * 128 + (tid & 7) * 16, SENT4);
#pragma unroll
      for (int t = 0; t < 16; ++t) cf[16 + t] = *(LAS v4u*)(L + L_TILE + (rB + i16) * 1024 + (t >> 2) * 256 + (((4 * (t & 3) + g) ^ sw) * 16));
      WAIT_LGKM0();
      BAR();
      f32x4 s0 = Z4, s1 = Z4;
      v4u qf[16];
#pragma unroll
      for (int t = 0; t < 16; ++t) qf[t] = *(LAS v4u*)(L + qr + t * 64);
      __builtin_amdgcn_sched_barrier(0);
#pragma unroll
      for (int t = 0; t < 16; ++t) { s0 = mfma(cf[t], qf[t], s0); s1 = mfma(cf[16 + t], qf[t], s1); }
      const float mx = fmaxf(fmaxf(fmaxf(s0.x, s0.y), fmaxf(s0.z, s0.w)), fmaxf(fmaxf(s1.x, s1.y), fmaxf(s1.z, s1.w)));
      const float mc = __builtin_ceilf(maxx32(maxx16(mx)));
      float p[8];
#pragma unroll
      for (int j = 0; j < 4; ++j) { p[j] = ex2(s0[j] - mc); p[4 + j] = ex2(s1[j] - mc); }
      *(LAS v2u*)(L + pwA) = v2u{pk2(p[0], p[1]), pk2(p[2], p[3])};
      *(LAS v2u*)(L + pwB) = v2u{pk2(p[4], p[5]), pk2(p[6], p[7])};
      const float ls = addx32(addx16(((p[0] + p[1]) + (p[2] + p[3])) + ((p[4] + p[5]) + (p[6] + p[7]))));
      if (g == 0) { *(LAS int*)(L + mcw) = (int)mc; *(LAS float*)(L + mcw + 16) = ls; }
      BAR();
      if (w == 0) {
        const v4i mo = *(LAS v4i*)(L + mcr);
        const f32x4 lo = *(LAS f32x4*)(L + mcr + 16);
        const int M = imax_(imax_(mo.x, mo.y), imax_(mo.z, mo.w));
        f32x4 f;
#pragma unroll
        for (int j = 0; j < 4; ++j) f[j] = asf((127u - umin_((unsigned)(M - mo[j]), 127u)) << 23);
        if (lane < 16) st16(rs, o3s, v4u{(unsigned)M, tlo, asu(hsum4(f * lo)), thi});
      }
    } else {
      const int hi = r < 26 ? (r - 16) >> 1 : r - 21, mcp = 2 * hi + (xc & 1);
      const bool isM = !isS1 && hi < 10;
      if (isM) {
        const int j0 = 160 * jq + 40 * wq4 + g, jc = j0 + 4 * (i16 < 9 ? i16 : 9);
        const unsigned c0 = *(const GAS u16*)(P.c + jc), c1 = *(const GAS u16*)(P.c + 640 + jc);
        v4u wm[10];
#pragma unroll
        for (int t = 0; t < 10; ++t) wm[t] = ldg16(P.wmod + (size_t)(j0 + 4 * t) * 3840 + 1280 + 128 * mcp + 8 * i16);
        __builtin_amdgcn_sched_barrier(0);
        const v4u sl = diag_sel(lane, i16 >> 3);
        const unsigned sp = silu_pair(c0, c1);
        f32x4 d = mfma(diag_mk(dppu<DPP_BC + 0>(sp), sl), wm[0], Z4);
        d = mfma(diag_mk(dppu<DPP_BC + 1>(sp), sl), wm[1], d); d = mfma(diag_mk(dppu<DPP_BC + 2>(sp), sl), wm[2], d);
        d = mfma(diag_mk(dppu<DPP_BC + 3>(sp), sl), wm[3], d); d = mfma(diag_mk(dppu<DPP_BC + 4>(sp), sl), wm[4], d);
        d = mfma(diag_mk(dppu<DPP_BC + 5>(sp), sl), wm[5], d); d = mfma(diag_mk(dppu<DPP_BC + 6>(sp), sl), wm[6], d);
        d = mfma(diag_mk(dppu<DPP_BC + 7>(sp), sl), wm[7], d); d = mfma(diag_mk(dppu<DPP_BC + 8>(sp), sl), wm[8], d);
        d = mfma(diag_mk(dppu<DPP_BC + 9>(sp), sl), wm[9], d);
        *(LAS f32x4*)(L + LA_MISC + (wq4 * 64 + lane) * 16) = d;
      }
      {
        tlo = __builtin_amdgcn_readfirstlane(ep.x) + 1u; thi = ~tlo; tb = tlo & 0xFFu;
      }
      BAR();
      if (isM && w == 7) {
        const LAS f32x4* rp = (const LAS f32x4*)(L + LA_MISC) + lane;
        const f32x4 sm = (rp[0] + rp[64]) + (rp[128] + rp[192]);
        st16(rs, WS_MR + ((jq * 2 + (g >> 1)) * 2560 + 128 * mcp + 8 * i16 + 4 * (g & 1)) * 4, tag4(sm, tb));
      }
      const int o = WS_H2 + (((b * 32 + 16 * hg + 4 * wq4 + (g >> 1)) * 2 + (g & 1)) * 512 + 8 * i16) * 2;
      int off[8] = {o, o + 256, o + 512, o + 768, o + 4096, o + 4352, o + 4608, o + 4864};
      v4u sel = diag_mk(((g >> 1) == (i16 >> 3)) ? 0x3F80u : 0u, diag_sel(lane, 0));
      unsigned qw = LA_Q + (4 * wq4 + (g >> 1)) * 1040 + (8 * i16 + 4 * (g & 1)) * 2;
      unsigned prd = LA_P + i16 * 272 + g * 16;
      int o3[2]; o3[0] = WS_H3 + ((16 * hg + 4 * g) * 4 + wq4) * 16384 + (b * 32 + s) * 256 + i16 * 8; o3[1] = o3[0] + 131072;
      unsigned ta0, ta1;
      {
        const unsigned q = i16 >> 2, p = lane & 3;
        const unsigned sw0 = (q << 2) | ((2 * g) & 3), sw1 = sw0 | 1u;
        ta0 = L_TILE + (8 * g + q) * 1024 + wq4 * 256 + (((p >> 1) ^ sw0) * 16) + 8 * (p & 1);
        ta1 = L_TILE + (8 * g + q + 4) * 1024 + wq4 * 256 + (((p >> 1) ^ sw1) * 16) + 8 * (p & 1);
      }
      PIN(ta0); PIN(ta1);
      PIN(off[0]); PIN(off[4]); PIN(sel); PIN(qw); PIN(prd); PIN(o3[0]); PIN(o3[1]); PIN(mcr);
      wait_tag64(rs, WS_HINT2 + (hg * 64 + 16 * wq4 + (lane >> 2)) * 128, tlo, thi);
      {
        v4u v[8];
        gather<8, 0>(rs, off, v, 0u);
        WAIT_VM0();
#pragma unroll
        for (int cg = 0; cg < 8; ++cg) {
          const f32x4 d = mfma(sel, v[cg], Z4);
          *(LAS v2u*)(L + qw + (cg >> 2) * 2080 + (cg & 3) * 256) = v2u{pk2(d.x, d.y), pk2(d.z, d.w)};
        }
      }
      BAR();
      nap<DLY_PV>();
#pragma unroll
      for (int tt = 0; tt < PV_PRE; ++tt)
#pragma unroll
        for (int kt = 0; kt < 4; ++kt) cf[tt * 4 + kt] = trfrag(L, (ta0 ^ (tt * 32)) + kt * 32768, (ta1 ^ (tt * 32)) + kt * 32768);
      BAR();
      const v4i mo = *(LAS v4i*)(L + mcr);
      v4u pfs[4];
#pragma unroll
      for (int kt = 0; kt < 4; ++kt) pfs[kt] = *(LAS v4u*)(L + prd + kt * 64);
#pragma unroll
      for (int tt = PV_PRE; tt < 8; ++tt)
#pragma unroll
        for (int kt = 0; kt < 4; ++kt) cf[tt * 4 + kt] = trfrag(L, (ta0 ^ (tt * 32)) + kt * 32768, (ta1 ^ (tt * 32)) + kt * 32768);
      __builtin_amdgcn_sched_barrier(0);
      const int M = imax_(imax_(mo.x, mo.y), imax_(mo.z, mo.w));
      v4u a[4];
#pragma unroll
      for (int kt = 0; kt < 4; ++kt) {
        const v4u pf = pfs[kt];
        const unsigned dl = umin_((unsigned)(M - mo[kt]), 255u);
        const u16x2 dd = __builtin_bit_cast(u16x2, dl * 0x00800080u);
#pragma unroll
        for (int j = 0; j < 4; ++j) { const unsigned e = pf[j]; a[kt][j] = __builtin_bit_cast(unsigned, __builtin_elementwise_sub_sat(__builtin_bit_cast(u16x2, e), dd)); }
      }
#pragma unroll
      for (int hf = 0; hf < 2; ++hf) {
        f32x4 d[4];
#pragma unroll
        for (int kt = 0; kt < 4; ++kt)
#pragma unroll
          for (int tt = 0; tt < 4; ++tt) d[tt] = mfma(a[kt], cf[(4 * hf + tt) * 4 + kt], kt == 0 ? Z4 : d[tt]);
#pragma unroll
        for (int j = 0; j < 4; ++j) st8(rs, o3[j >> 1] + (j & 1) * 65536 + hf * 128, v2u{nosent(pk2(d[0][j], d[1][j])), nosent(pk2(d[2][j], d[3][j]))});
      }
    }
    BAR();
    if (roleO) {
    {
      const u16* wo = P.wo + (size_t)(512 * kr) * 640 + 64 * ng;
      tile_dma<8>(wo, wo + 32, 1280, 64 * w, lane, Lb + L_TILE, 0);
    }
    LAS float* MS = (LAS float*)(L + L_TILEB + 28672);
    if (w == 0) {
      int off[1] = {WS_MC + (((lane >> 4) & 1) * 2560 + (lane >> 5) * 1280 + 64 * ng + i16 * 4) * 4};
      v4u v[1];
      gather<1, 1, 0, 4>(rs, off, v, tb);
      *(LAS f32x4*)(MS + lane * 4) = untag4(v[0]);
    }
    WAIT_VM0();
    BAR();
    v4u wf[3][4];
    tile_frags<2>(L, L_TILE, 64 * w, lane, wf);
    const int fo = (tid >> 1) & 127;
    float scl;
    {
      const float ga = MS[fo], cb = MS[128 + fo];
      scl = ga * guard_oc(1.f + cb);
    }
    int off[1] = {WS_H4 + (8 * kr + w) * 512 + (lane & 31) * 16};
    unsigned xbw = LT_MISC + 4096 + w * 1152 + (lane >> 3) * 144 + (lane & 7) * 16;
    unsigned xbr = LT_MISC + 4096 + w * 1152 + (i16 & 3) * 144 + g * 16;
    unsigned rw = LT_MISC + (((w >> 2) * 128 + lane) * 4 + (w & 3)) * 4;
    unsigned rr = LT_MISC + ((tid & 1) * 128 + fo) * 16;
    int o5 = WS_H5 + ((((2 * ng + ((fo >> 5) & 1)) * 8 + kr) * 2 + (fo >> 6)) * 32 + (fo & 31)) * 2;
    PIN(off[0]); PIN(xbw); PIN(xbr); PIN(rw); PIN(rr); PIN(o5); PIN(scl);
    WAIT_LGKM0();
    wait_tag64(rs, WS_HINT4 + (xc * 8 + (w & 6) + (lane >> 5)) * 128, tlo, thi);
    v4u v[1];
    gather<1, 0>(rs, off, v, 0u);
    if (tid == 0) st8(rs, WS_HINT5 + (ng * 8 + kr) * 128, v2u{tlo, thi});
    *(LAS v4u*)(L + xbw) = v[0];
    LDS_ORDER();
    const v4u a0 = *(LAS v4u*)(L + xbr), a1 = *(LAS v4u*)(L + xbr + 64);
    f32x4 acc[4];
#pragma unroll
    for (int nt = 0; nt < 4; ++nt) acc[nt] = mfma(a0, wf[0][nt], Z4);
#pragma unroll
    for (int nt = 0; nt < 4; ++nt) acc[nt] = mfma(a1, wf[1][nt], acc[nt]);
    const f32x4 e = g < 2 ? (g == 0 ? acc[0] : acc[1]) : (g == 2 ? acc[2] : acc[3]);
    *(LAS float*)(L + rw) = e.x + e.y;
    *(LAS float*)(L + rw + 1024) = e.z + e.w;
    BAR();
    if (w < 4) {
      const float z = opq(dadd<DPP_X1>(hsum4(*(LAS f32x4*)(L + rr))) * scl);
      const unsigned ov = nosent(pk2(z, dppf<DPP_X2>(z)));
      if ((lane & 3) == 0) st4(rs, o5, ov);
    }
    }
  }
  WAIT_VM0();
}

extern "C" void kernel_launch(void* const* d_in, const int* in_sizes, int n_in,
                              void* d_out, int out_size, void* d_ws, size_t ws_size,
                              hipStream_t stream) {
  Params p;
  p.x = (const u16*)d_in[0]; p.ckv = (const u16*)d_in[1]; p.wq = (const u16*)d_in[2]; p.wuk = (const u16*)d_in[3];
  p.wuv = (const u16*)d_in[4]; p.wo = (const u16*)d_in[5]; p.wg = (const u16*)d_in[7]; p.wu = (const u16*)d_in[8];
  p.wd = (const u16*)d_in[9]; p.c = (const u16*)d_in[10]; p.wmod = (const u16*)d_in[11];
  p.out = (u16*)d_out; p.ws = (unsigned char*)d_ws;
  mk_kernel<<<256, 512, 0, stream>>>(p);
}
```
